```python
import torch

def _softmax(x, axis=-1):
    return torch.softmax(x, dim=axis)
def _gelu(x):
    return torch.nn.functional.gelu(x, approximate="tanh")
def _silu(x):
    return torch.nn.functional.silu(x)
def _sigmoid(x):
    return torch.sigmoid(x)
def _layernorm(x, axis=-1, eps=1e-5):
    m = x.mean(dim=axis, keepdim=True)
    v = x.var(dim=axis, keepdim=True, unbiased=False)
    return (x - m) / torch.sqrt(v + eps)
def _scatter_add(tgt, idx, src, axis=0):
    return tgt.clone().index_add_(axis, idx, src)
def _segment_sum(src, seg, n):
    out = torch.zeros((n,) + src.shape[1:], dtype=src.dtype)
    return out.index_add_(0, seg, src)
def _pad(x, pw):
    flat = tuple(v for pair in reversed(pw) for v in pair)
    return torch.nn.functional.pad(x, flat)

kernel_name = 'parallel_sum__grouped_kv_mqa__glu_gated_reglu__prompt'

def setup_inputs():

    _w = (lambda a: (lambda t: t.to(torch.bfloat16) if t.is_floating_point() else t)(torch.as_tensor(a)))
    def _A_setup():
        B, S, D, H = 1, 2048, 576, 32
        Dh = D // H
        return {
            "x": _w((lambda *s: torch.randn(*s, dtype=torch.bfloat16) * 0.05)(B, S, D)),
            "wq": _w((lambda *s: torch.randn(*s, dtype=torch.bfloat16) * 0.05)(D, H * Dh)),
            "wk": _w((lambda *s: torch.randn(*s, dtype=torch.bfloat16) * 0.05)(D, Dh)),
            "wv": _w((lambda *s: torch.randn(*s, dtype=torch.bfloat16) * 0.05)(D, Dh)),
            "wo": _w((lambda *s: torch.randn(*s, dtype=torch.bfloat16) * 0.05)(H * Dh, D)),
        }
    _A_in = _A_setup()

    def _B_setup():
        B, S, D, F = 1, 2048, 576, 4608
        return {
            "x": _w((lambda *s: torch.randn(*s, dtype=torch.bfloat16) * 0.05)(B, S, D)),
            "W_gate": _w((lambda *s: torch.randn(*s, dtype=torch.bfloat16) * 0.4)(D, F) * (2.0 / D) ** 0.5),
            "W_up": _w((lambda *s: torch.randn(*s, dtype=torch.bfloat16) * 0.4)(D, F) * (2.0 / D) ** 0.5),
            "W_down": _w((lambda *s: torch.randn(*s, dtype=torch.bfloat16) * 0.4)(F, D) * (1.0 / F) ** 0.5),
        }
    _B_in = _B_setup()

    return {
        "A_x": _A_in["x"],
        "A_wq": _A_in["wq"],
        "A_wk": _A_in["wk"],
        "A_wv": _A_in["wv"],
        "A_wo": _A_in["wo"],
        "B_x": _B_in["x"],
        "B_W_gate": _B_in["W_gate"],
        "B_W_up": _B_in["W_up"],
        "B_W_down": _B_in["W_down"],
    }

def reference(A_x, A_wq, A_wk, A_wv, A_wo, B_x, B_W_gate, B_W_up, B_W_down):
    def _A_ref(x, wq, wk, wv, wo):
        B, S, D = x.shape
        H = 32
        Dh = D // H
        q = (lambda x, *d: x.permute(*d))((x @ wq).reshape(B, S, H, Dh), 0, 2, 1, 3)
        k = x @ wk
        v = x @ wv
        scores = torch.einsum("bhqd,bkd->bhqk", q, k) * (Dh ** -0.5)
        p = _softmax(scores, axis=-1)
        o = torch.einsum("bhqk,bkd->bhqd", p, v)
        return (lambda x, *d: x.permute(*d))(o, 0, 2, 1, 3).reshape(B, S, H * Dh) @ wo

    def _B_ref(x, W_gate, W_up, W_down):
        gate = torch.einsum("bsd,df->bsf", x, W_gate)
        up = torch.einsum("bsd,df->bsf", x, W_up)
        gate = gate * (lambda x, dt: x.to(dt))(gate > 0, gate.dtype)
        h = gate * up
        return torch.einsum("bsf,fd->bsd", h, W_down)

    x = A_x
    xn = _layernorm(x, axis=-1)
    return x + _A_ref(xn, A_wq, A_wk, A_wv, A_wo) + _B_ref(xn, B_W_gate, B_W_up, B_W_down)
```

```cpp
#include <hip/hip_runtime.h>
#include <hip/hip_bf16.h>
typedef unsigned short u16;
__device__ __forceinline__ float b2f(u16 v){ return __uint_as_float(((unsigned)v)<<16); }
__device__ __forceinline__ u16 f2b(float f){ unsigned u=__float_as_uint(f); u+=0x7fffu+((u>>16)&1u); return (u16)(u>>16); }
__device__ __forceinline__ float rb(float f){ return b2f(f2b(f)); }
constexpr int S=2048,D=576,H=32,DH=18,F=4608;
__global__ void ln_k(const u16* x,u16* xn){
  int row=blockIdx.x; __shared__ float red[256]; int t=threadIdx.x;
  float s=0; for(int i=t;i<D;i+=256)s+=b2f(x[row*D+i]); red[t]=s; __syncthreads();
  for(int o=128;o>0;o>>=1){ if(t<o)red[t]+=red[t+o]; __syncthreads(); }
  float mean=red[0]/D; __syncthreads();
  float v=0; for(int i=t;i<D;i+=256){float d=b2f(x[row*D+i])-mean; v+=d*d;} red[t]=v; __syncthreads();
  for(int o=128;o>0;o>>=1){ if(t<o)red[t]+=red[t+o]; __syncthreads(); }
  float var=red[0]/D; float mb=rb(mean), vb=rb(var); float sd=rb(sqrtf(rb(vb+1e-5f)));
  for(int i=t;i<D;i+=256){ float d=rb(b2f(x[row*D+i])-mb); xn[row*D+i]=f2b(d/sd); }
}
__global__ void gemm_k(const u16* A,const u16* B,u16* C,int M,int N,int K){
  int n=blockIdx.x*64+(threadIdx.x&63); int m=blockIdx.y*4+(threadIdx.x>>6); if(n>=N||m>=M)return;
  float acc=0; for(int k=0;k<K;++k)acc+=b2f(A[(size_t)m*K+k])*b2f(B[(size_t)k*N+n]); C[(size_t)m*N+n]=f2b(acc);
}
__global__ void h_k(const u16* g,const u16* u,u16* h,int n){ int i=blockIdx.x*256+threadIdx.x; if(i<n){ float gg=b2f(g[i]); gg=gg>0?gg:0.f; h[i]=f2b(gg*b2f(u[i])); } }
__global__ void attn_k(const u16* q,const u16* k,const u16* v,u16* o){
  int idx=blockIdx.x*64+threadIdx.x; int row=idx/H, h=idx%H;
  float qq[DH]; for(int d=0;d<DH;++d)qq[d]=b2f(q[row*D+h*DH+d]);
  float m=-1e30f,l=0,acc[DH]; for(int d=0;d<DH;++d)acc[d]=0;
  for(int j=0;j<S;++j){ float s=0; for(int d=0;d<DH;++d)s+=qq[d]*b2f(k[j*DH+d]); s*=0.23570226039551584f;
    float mn=fmaxf(m,s); float a=__expf(m-mn), p=__expf(s-mn); l=l*a+p; for(int d=0;d<DH;++d)acc[d]=acc[d]*a+p*b2f(v[j*DH+d]); m=mn; }
  for(int d=0;d<DH;++d)o[row*D+h*DH+d]=f2b(acc[d]/l);
}
__global__ void fin_k(const u16* x,const u16* a,const u16* b,u16* out,int n){ int i=blockIdx.x*256+threadIdx.x; if(i<n) out[i]=f2b(rb(b2f(x[i])+b2f(a[i]))+b2f(b[i])); }
extern "C" void kernel_launch(void* const* d_in, const int* in_sizes, int n_in, void* d_out, int out_size, void* d_ws, size_t ws_size, hipStream_t stream) {
  const u16* x=(const u16*)d_in[0]; const u16* wq=(const u16*)d_in[1]; const u16* wk=(const u16*)d_in[2]; const u16* wv=(const u16*)d_in[3]; const u16* wo=(const u16*)d_in[4];
  const u16* wg=(const u16*)d_in[6]; const u16* wu=(const u16*)d_in[7]; const u16* wd=(const u16*)d_in[8];
  u16* ws=(u16*)d_ws; u16* xn=ws; u16* q=xn+S*D; u16* k=q+S*D; u16* v=k+S*32; u16* o=v+S*32; u16* a=o+S*D; u16* b=a+S*D; u16* g=b+S*D; u16* u=g+(size_t)S*F; u16* h=u+(size_t)S*F;
  ln_k<<<S,256,0,stream>>>(x,xn);
  gemm_k<<<dim3(D/64,S/4),256,0,stream>>>(xn,wq,q,S,D,D);
  gemm_k<<<dim3(1,S/4),256,0,stream>>>(xn,wk,k,S,DH,D);
  gemm_k<<<dim3(1,S/4),256,0,stream>>>(xn,wv,v,S,DH,D);
  attn_k<<<S*H/64,64,0,stream>>>(q,k,v,o);
  gemm_k<<<dim3(D/64,S/4),256,0,stream>>>(o,wo,a,S,D,D);
  gemm_k<<<dim3(F/64,S/4),256,0,stream>>>(xn,wg,g,S,F,D);
  gemm_k<<<dim3(F/64,S/4),256,0,stream>>>(xn,wu,u,S,F,D);
  h_k<<<(S*F+255)/256,256,0,stream>>>(g,u,h,S*F);
  gemm_k<<<dim3(D/64,S/4),256,0,stream>>>(h,wd,b,S,D,F);
  fin_k<<<(S*D+255)/256,256,0,stream>>>(x,a,b,(u16*)d_out,S*D);
}
```

```cpp
#include <hip/hip_runtime.h>
#include <stdint.h>
#include <utility>

#define XMAP 2
#define FFN_PRIO 1
#define DI __device__ __forceinline__
#define LAS __attribute__((address_space(3)))
typedef unsigned short u16;
typedef __bf16 bf16x8 __attribute__((ext_vector_type(8)));
typedef __bf16 bf16x2 __attribute__((ext_vector_type(2)));
typedef short s16x4 __attribute__((ext_vector_type(4)));
typedef float f32x2 __attribute__((ext_vector_type(2)));
typedef float f32x4 __attribute__((ext_vector_type(4)));
typedef float f32x16 __attribute__((ext_vector_type(16)));
typedef unsigned u32x2 __attribute__((ext_vector_type(2)));
typedef unsigned u32x4 __attribute__((ext_vector_type(4)));
typedef LAS char* lptr;

constexpr int SEQ = 2048, DM = 576, FF = 4608;
constexpr unsigned WS_K1 = 0, WS_K2 = 65536, WS_V = WS_K2 + 64 * 512, WS_KV_END = WS_V + 128 * 608;
constexpr unsigned WS_XN = 262144, XN_BYTES = SEQ * DM * 2;
constexpr size_t WS_PART = 4u << 20;
constexpr unsigned SLOT = 12288, L_CNT = 0, L_RING = 4096, L_H = L_RING + 6 * SLOT, L_Q = L_H + 57344, L_O = L_H + 73728, O_STRIDE = 96, L_X = 16384, L_XCH = L_RING + 4 * SLOT, LDS_BYTES = 163840;
constexpr unsigned BBUF = 43008;
static_assert(L_O + 128 * O_STRIDE <= LDS_BYTES && L_X + 128 * 1152 <= LDS_BYTES && L_RING + 3 * BBUF + 1024 <= L_O, "LDS map");
constexpr unsigned OOB = 0x40000000u;
constexpr float QSCALE = 0.23570226039551584f * 1.4426950408889634f;

#define SBAR() __builtin_amdgcn_sched_barrier(0)
#define PIN(x) asm volatile("" : "+v"(x))
#define MFMA32(a, b, c) __builtin_amdgcn_mfma_f32_32x32x16_bf16(__builtin_bit_cast(bf16x8, a), __builtin_bit_cast(bf16x8, b), c, 0, 0, 0)
#define MFMA16(a, b, c) __builtin_amdgcn_mfma_f32_16x16x32_bf16(__builtin_bit_cast(bf16x8, a), __builtin_bit_cast(bf16x8, b), c, 0, 0, 0)
#define WAITV(n) asm volatile("s_waitcnt vmcnt(" #n ")" ::: "memory")
#define WAITL0() asm volatile("s_waitcnt lgkmcnt(0)" ::: "memory")
#define FENCE() asm volatile("" ::: "memory")
#define BARRIER() asm volatile("s_waitcnt lgkmcnt(0)\n\ts_barrier" ::: "memory")
#define ZERO16 ((f32x16){})

DI unsigned cvtpk(float lo, float hi) { f32x2 v = {lo, hi}; return __builtin_bit_cast(unsigned, __builtin_convertvector(v, bf16x2)); }
DI float bfr(float f) { return __uint_as_float(cvtpk(f, 0.f) << 16); }
DI float dot2(unsigned a, unsigned b, float c) { return __builtin_amdgcn_fdot2_f32_bf16(__builtin_bit_cast(bf16x2, a), __builtin_bit_cast(bf16x2, b), c, false); }
DI float blo(unsigned v) { return __uint_as_float(v << 16); }
DI float bhi(unsigned v) { return __uint_as_float(v & 0xffff0000u); }
DI u32x2 trread(lptr p) { return __builtin_bit_cast(u32x2, __builtin_amdgcn_ds_read_tr16_b64_v4i16((LAS s16x4*)p)); }
DI u32x4 trfrag(lptr p, unsigned second) { u32x2 a = trread(p), b = trread(p + second); return (u32x4){a[0], a[1], b[0], b[1]}; }
DI __amdgpu_buffer_rsrc_t mkrsrc(const void* p, unsigned bytes) { return __builtin_amdgcn_make_buffer_rsrc((void*)p, (short)0, (int)bytes, 0x00020000); }
DI void dma16(unsigned ldsaddr, unsigned voff, __amdgpu_buffer_rsrc_t rs, unsigned soff) {
    unsigned keep;
    asm volatile("s_mov_b32 %0, m0\n\ts_mov_b32 m0, %1\n\ts_nop 0\n\tbuffer_load_dwordx4 %2, %3, %4 offen lds\n\ts_mov_b32 m0, %0"
                 : "=&s"(keep) : "s"(ldsaddr), "v"(voff), "s"(rs), "s"(soff) : "memory");
}
DI float swap32_add(float v) { auto r = __builtin_amdgcn_permlane32_swap(__float_as_uint(v), __float_as_uint(v), false, false); return __uint_as_float(r[0]) + __uint_as_float(r[1]); }
DI float swap32_max(float v) { auto r = __builtin_amdgcn_permlane32_swap(__float_as_uint(v), __float_as_uint(v), false, false); return fmaxf(__uint_as_float(r[0]), __uint_as_float(r[1])); }
DI void lds_add1(unsigned addr_v, unsigned one_v) { asm volatile("s_mov_b64 exec, 1\n\tds_add_u32 %0, %1\n\ts_mov_b64 exec, -1" :: "v"(addr_v), "v"(one_v) : "memory"); }
DI unsigned lds_ld(lptr p) { return __hip_atomic_load((LAS unsigned*)p, __ATOMIC_RELAXED, __HIP_MEMORY_SCOPE_WORKGROUP); }
DI void lds_wait_ge(lptr p, unsigned v) { while ((unsigned)__builtin_amdgcn_readfirstlane(lds_ld(p)) < v) __builtin_amdgcn_s_sleep(1); FENCE(); }

#define STAMP(i) do {} while (0)
#define STAMPR(i) do {} while (0)
DI void ln_scalars(float sum, float sq, float& mb, float& rinv) {
    const float mean = sum * (1.f / DM); float var = sq * (1.f / DM) - mean * mean; var = fmaxf(var, 0.f);
    mb = bfr(mean); const float sd = bfr(__builtin_amdgcn_sqrtf(bfr(bfr(var) + 1e-5f))); rinv = __builtin_amdgcn_rcpf(sd);
}
DI unsigned ln_apply(unsigned v, float rinv, float c) { return cvtpk(__builtin_fmaf(blo(v), rinv, c), __builtin_fmaf(bhi(v), rinv, c)); }

constexpr unsigned KV_XS = 1168, KV_W = 16 * KV_XS, KV_LDS = KV_W + 576 * 80 + 128;
template <class SYNC>
DI void kv_job(lptr lds, int t256, int mt, int s, const u16* __restrict__ x, const u16* __restrict__ wk, const u16* __restrict__ wv, char* ws, SYNC sync) {
    const int lane = t256 & 63, a = __builtin_amdgcn_readfirstlane(t256 >> 6);
    {
        const unsigned* wk32 = (const unsigned*)wk; const unsigned* wv32 = (const unsigned*)wv;
        unsigned bk[21], bv[21];
#pragma unroll
        for (int i = 0; i < 21; ++i) { const int idx = t256 + 256 * i; const int id2 = idx < 5184 ? idx : 5183; bk[i] = wk32[id2]; bv[i] = wv32[id2]; }
        const int row = 128 * mt + 8 * s + 2 * a + (lane >> 5), c = lane & 31;
        const unsigned* xr = (const unsigned*)(x + (size_t)row * DM);
        unsigned xv[9];
#pragma unroll
        for (int j = 0; j < 9; ++j) xv[j] = xr[c + 32 * j];
        float sum = 0.f, sq = 0.f;
#pragma unroll
        for (int j = 0; j < 9; ++j) { sum = dot2(xv[j], 0x3F803F80u, sum); sq = dot2(xv[j], xv[j], sq); }
#pragma unroll
        for (int m = 1; m < 32; m <<= 1) { sum += __shfl_xor(sum, m, 64); sq += __shfl_xor(sq, m, 64); }
        float mb, rinv; ln_scalars(sum, sq, mb, rinv); const float cc = -mb * rinv;
#pragma unroll
        for (int j = 0; j < 9; ++j) *(LAS unsigned*)(lds + (2 * a + (lane >> 5)) * KV_XS + 4 * (c + 32 * j)) = ln_apply(xv[j], rinv, cc);
#pragma unroll
        for (int i = 0; i < 21; ++i) { const int idx = t256 + 256 * i; if (idx < 5184) { const int r = idx / 9, cd = idx - 9 * r;
            *(LAS unsigned*)(lds + KV_W + r * 80 + 4 * cd) = bk[i]; *(LAS unsigned*)(lds + KV_W + r * 80 + 40 + 4 * cd) = bv[i]; } }
    }
    sync();
    {
        const __amdgpu_buffer_rsrc_t rx = mkrsrc(ws + WS_XN, XN_BYTES);
#pragma unroll
        for (int i = 0; i < 3; ++i) { const int id = t256 + 256 * i; if (id < 576) { const int c = id >> 3, rr = id & 7, row = 128 * mt + 8 * s + rr;
            const u32x4 v = *(LAS u32x4*)(lds + rr * KV_XS + c * 16);
            __builtin_amdgcn_raw_buffer_store_b128(v, rx, (((row >> 5) * 36 + (c >> 1)) * 64 + (row & 31) + 32 * (c & 1)) * 16, 0, 16); } }
    }
    const int i16 = lane & 15, g = lane >> 4, q = i16 >> 2, p = i16 & 3;
    const unsigned c0b = a == 0 ? 0u : a == 1 ? 32u : a == 2 ? 40u : 72u;
    lptr wp = lds + KV_W + (8 * g + q) * 80 + c0b + 8 * p;
    lptr xp = lds + (i16 & 7) * KV_XS + 16 * g;
    f32x4 acc = {0.f, 0.f, 0.f, 0.f};
    if (a < 2) {
#pragma unroll
        for (int t = 0; t < 18; ++t) { const u32x4 wf = trfrag(wp + t * 32 * 80, 4 * 80); const u32x4 xf = *(LAS u32x4*)(xp + t * 64); acc = MFMA16(wf, xf, acc); }
    } else {
#pragma unroll
        for (int t = 0; t < 18; ++t) { const u32x4 wf = trfrag(wp + t * 32 * 80, 4 * 80); const u32x4 xf = *(LAS u32x4*)(xp + t * 64); acc = MFMA16(xf, wf, acc); }
    }
    const __amdgpu_buffer_rsrc_t rs = mkrsrc(ws, WS_KV_END);
    const int ky0 = 128 * mt + 8 * s;
    const unsigned p01 = cvtpk(acc[0], acc[1]), p23 = cvtpk(acc[2], acc[3]);
    if (a == 0) { if (i16 < 8) { const int ky = ky0 + i16; __builtin_amdgcn_raw_buffer_store_b64((u32x2){p01, p23}, rs, WS_K1 + (ky >> 5) * 1024 + ((ky & 31) + 32 * (g & 1)) * 16 + 8 * (g >> 1), 0, 16); } }
    else if (a == 1) { if (i16 < 8 && g == 0) { const int ky = ky0 + i16; __builtin_amdgcn_raw_buffer_store_b128((u32x4){p01, 0x00003F80u, 0u, 0u}, rs, WS_K2 + (ky >> 5) * 512 + (ky & 31) * 16, 0, 16); } }
    else { const int c = (a == 2 ? 0 : 16) + i16; const bool ones = (c == 18);
        if (g < 2 && c <= 18) __builtin_amdgcn_raw_buffer_store_b64(ones ? (u32x2){0x3F803F80u, 0x3F803F80u} : (u32x2){p01, p23}, rs, WS_V + (ky0 >> 4) * 608 + g * 304 + c * 16 + 8 * (s & 1), 0, 16); }
}

__global__ __launch_bounds__(256) void kv_kernel(const u16* __restrict__ x, const u16* __restrict__ wk, const u16* __restrict__ wv, char* ws) {
    extern __shared__ __attribute__((aligned(16))) char smem[];
    kv_job((lptr)smem, threadIdx.x, blockIdx.x >> 4, blockIdx.x & 15, x, wk, wv, ws, [] { __syncthreads(); });
}

struct Params { const u16* x; const u16* wq; const u16* wo; const u16* wg; const u16* wu; const u16* wd; char* ws; };

struct Tgt { __amdgpu_buffer_rsrc_t rs[3]; unsigned base[3], ld[3], jstr; unsigned vo; };
struct Ffn {
    u32x4 xf[36];
    u32x4 ag[3], au[3];
    lptr rb0, rb1;
    Tgt cur, nxt;
    int w, lane;
    lptr lds; unsigned sync_target; unsigned cnt_v, one_v;
};
template <int Q> DI void ffn_dma(const Tgt& t, int j) { dma16(t.ld[Q] + j * SLOT, t.vo, t.rs[Q], t.base[Q] + j * t.jstr); }
template <int T, int U> DI void ffn_read1(Ffn& f) {
    constexpr int j = T / 6, kk = T % 6; lptr b = (j < 3 ? f.rb0 : f.rb1) + (j % 3) * SLOT + kk * 1024 + U * 6144;
    if constexpr (U) f.au[T % 3] = trfrag(b, 256); else f.ag[T % 3] = trfrag(b, 256);
}
template <int T> DI void ffn_issue(Ffn& f) {
    constexpr int j = T / 6, kk = T % 6;
    if constexpr (kk == 4 || kk == 0 || kk == 2) {
        constexpr int q = kk == 4 ? 0 : kk == 0 ? 1 : 2; constexpr int m = (kk == 4 ? j : j - 1) + 5;
        if constexpr (m < 6) ffn_dma<q>(f.cur, m); else ffn_dma<q>(f.nxt, m - 6);
    }
}
#define SYNC_ON true
template <int T> DI void ffn_step(Ffn& f, f32x16& g, f32x16& u, unsigned& polled) {
    constexpr int kk = T % 6;
    if constexpr (SYNC_ON && kk == 1) {
        WAITV(8);
        lds_add1(f.cnt_v, f.one_v);
        f.sync_target += 4;
    }
    if constexpr (SYNC_ON && kk == 3) polled = lds_ld(f.lds + L_CNT);
    if constexpr (SYNC_ON && kk == 4) {
        if (__builtin_expect((unsigned)__builtin_amdgcn_readfirstlane(polled) < f.sync_target, 0)) {
            while ((unsigned)__builtin_amdgcn_readfirstlane(lds_ld(f.lds + L_CNT)) < f.sync_target) {} }
        FENCE();
    }
    ffn_read1<(T + 2) % 36, 0>(f);
    SBAR();
    if constexpr (T == 0) g = MFMA32(f.ag[0], f.xf[0], ZERO16); else g = MFMA32(f.ag[T % 3], f.xf[T], g);
    ffn_issue<T>(f);
    SBAR();
    ffn_read1<(T + 2) % 36, 1>(f);
    SBAR();
    if constexpr (T == 0) u = MFMA32(f.au[0], f.xf[0], ZERO16); else u = MFMA32(f.au[T % 3], f.xf[T], u);
    SBAR();
}
template <int... Ts> DI void ffn_chunk_(Ffn& f, f32x16& g, f32x16& u, std::integer_sequence<int, Ts...>) { unsigned polled = 0; (ffn_step<Ts>(f, g, u, polled), ...); }

DI void ffn_path(lptr lds, const Params& P, int mt, int s, int w, int lane) {
    char* DBGWS = P.ws; (void)DBGWS;
    Ffn f; f.lds = lds; f.w = w; f.lane = lane;
    const int h = lane >> 5, g4 = lane >> 4, q = (lane & 15) >> 2, p = lane & 3;
    const unsigned ring = (unsigned)(uintptr_t)(lds + L_RING);
    const __amdgpu_buffer_rsrc_t rsG = mkrsrc(P.wg, DM * FF * 2), rsU = mkrsrc(P.wu, DM * FF * 2), rs1 = mkrsrc(w < 2 ? P.wg : P.wu, DM * FF * 2);
    const unsigned vo_w = (lane >> 2) * 9216 + (lane & 3) * 16;
    unsigned row[3];
    { const __amdgpu_buffer_rsrc_t rsQ = mkrsrc(P.wq, DM * DM * 2);
#pragma unroll
      for (int i = 0; i < 3; ++i) { const int pi = w + 4 * i; const bool up = pi >= 6; const int pr = up ? pi - 6 : pi;
          row[i] = 16u * pr; f.cur.ld[i] = ring + (up ? 6144u : 0u) + pr * 1024u; f.cur.rs[i] = rsQ; f.cur.base[i] = row[i] * 1152u + (unsigned)s * 72u + (up ? 36u : 0u);
          f.nxt.ld[i] = f.cur.ld[i]; f.nxt.base[i] = row[i] * 9216u + (unsigned)s * 576u; }
      f.cur.jstr = 96u * 1152u; f.cur.vo = (lane >> 2) * 1152 + (lane & 3) * 16;
      f.nxt.rs[0] = rsG; f.nxt.rs[1] = rs1; f.nxt.rs[2] = rsU; f.nxt.jstr = 96u * 9216u; f.nxt.vo = vo_w; }
#pragma unroll
    for (int j = 0; j < 4; ++j) { ffn_dma<0>(f.cur, j); ffn_dma<1>(f.cur, j); ffn_dma<2>(f.cur, j); }
    ffn_dma<0>(f.cur, 4);
    STAMP(26);
    { const __amdgpu_buffer_rsrc_t rx = mkrsrc(P.ws + WS_XN, XN_BYTES); const unsigned vo = lane * 16, so = (unsigned)(4 * mt + w) * 36u * 1024u;
#pragma unroll
      for (int t = 0; t < 36; ++t) f.xf[t] = __builtin_amdgcn_raw_buffer_load_b128(rx, vo, so + t * 1024, 0); }
    f.rb0 = lds + L_RING + (8 * h + q) * 64 + (g4 & 1) * 32 + 8 * p; f.rb1 = f.rb0 + 3 * SLOT; PIN(f.rb1);
    f.cnt_v = (unsigned)(uintptr_t)(lds + L_CNT); f.one_v = 1u; PIN(f.cnt_v); PIN(f.one_v);
    f.sync_target = 4;
    STAMP(27);
    WAITV(0);
    STAMP(28);
#pragma unroll
    for (int t = 0; t < 36; ++t) PIN(f.xf[t]);
    lds_add1(f.cnt_v, f.one_v);
    lds_wait_ge(lds + L_CNT, 4);
    STAMP(2);
    ffn_read1<0, 0>(f); ffn_read1<0, 1>(f); ffn_read1<1, 0>(f); ffn_read1<1, 1>(f);
    __builtin_amdgcn_s_setprio(FFN_PRIO);
    lptr hw = lds + L_H + w * 2048 + lane * 16;
    for (int c = 0; c < 10; ++c) {
        f32x16 g, u;
        ffn_chunk_(f, g, u, std::make_integer_sequence<int, 36>{});
        if (c == 0) {
            const bool lo = lane < 32;
#pragma unroll
            for (int a = 0; a < 2; ++a) { const f32x16& qq = a ? u : g; u32x4 f0, f1;
#pragma unroll
                for (int i = 0; i < 4; ++i) f0[i] = cvtpk(qq[2 * i] * QSCALE, qq[2 * i + 1] * QSCALE);
                f1 = (u32x4){lo ? cvtpk(qq[8] * QSCALE, qq[9] * QSCALE) : 0u, 0u, 0u, 0u};
                *(LAS u32x4*)(lds + L_Q + (w * 2 + a) * 2048 + lane * 16) = f0;
                *(LAS u32x4*)(lds + L_Q + (w * 2 + a) * 2048 + 1024 + lane * 16) = f1; }
            WAITL0();
            lds_add1(f.cnt_v + 4, f.one_v);
        } else {
            if (c == 8) lds_wait_ge(lds + L_CNT + 8, 4);
            u32x4 w0, w1;
#pragma unroll
            for (int i = 0; i < 4; ++i) { w0[i] = cvtpk(__builtin_fmaxf(g[2 * i], 0.f) * u[2 * i], __builtin_fmaxf(g[2 * i + 1], 0.f) * u[2 * i + 1]);
                                          w1[i] = cvtpk(__builtin_fmaxf(g[8 + 2 * i], 0.f) * u[8 + 2 * i], __builtin_fmaxf(g[9 + 2 * i], 0.f) * u[9 + 2 * i]); }
            *(LAS u32x4*)(hw + (c - 1) * 8192) = w0; *(LAS u32x4*)(hw + (c - 1) * 8192 + 1024) = w1;
        }
        f.cur = f.nxt;
        if (c < 8) {
#pragma unroll
            for (int i = 0; i < 3; ++i) f.nxt.base[i] += 64u;
        } else if (c == 8) {
            const __amdgpu_buffer_rsrc_t rsD = mkrsrc(P.wd, FF * DM * 2);
#pragma unroll
            for (int i = 0; i < 3; ++i) { f.nxt.rs[i] = rsD; f.nxt.ld[i] = ring + 1024u * (w + 4 * i); f.nxt.base[i] = (288u * s + 8u * (w + 4 * i)) * 1152u; }
            f.nxt.jstr = 96u * 1152u; f.nxt.vo = (lane >> 3) * 1152 + (((lane & 7) ^ (((lane >> 4) & 1) << 2)) * 16);
        }
        STAMP(3 + c);
    }
    __builtin_amdgcn_s_setprio(0);
    WAITV(0);
}

struct KVf { u32x4 k1, k2, v0, v1; };
#define EX(x) __builtin_amdgcn_exp2f(x)
DI void att_slot(f32x16& Sc, f32x16& Sn, u32x4& pup, const u32x4& k1, const u32x4& k2, const u32x4& q1, const u32x4& q2,
                 const u32x4& v1p, f32x16& Op, const u32x4& v0c, f32x16& Oc) {
    u32x4 plo;
    pup[2] = cvtpk(Sn[12], Sn[13]); pup[3] = cvtpk(Sn[14], Sn[15]); PIN(pup); SBAR();
    Sn = MFMA32(k1, q1, ZERO16);
    Sc[0] = EX(Sc[0]); Sc[1] = EX(Sc[1]); Sc[2] = EX(Sc[2]); Sc[3] = EX(Sc[3]); PIN(Sc); SBAR();
    Op = MFMA32(v1p, pup, Op);
    Sc[4] = EX(Sc[4]); Sc[5] = EX(Sc[5]); Sc[6] = EX(Sc[6]); Sc[7] = EX(Sc[7]); plo[0] = cvtpk(Sc[0], Sc[1]); plo[1] = cvtpk(Sc[2], Sc[3]); PIN(Sc); PIN(plo); SBAR();
    Sn = MFMA32(k2, q2, Sn);
    Sc[8] = EX(Sc[8]); Sc[9] = EX(Sc[9]); Sc[10] = EX(Sc[10]); Sc[11] = EX(Sc[11]); plo[2] = cvtpk(Sc[4], Sc[5]); plo[3] = cvtpk(Sc[6], Sc[7]); PIN(Sc); PIN(plo); SBAR();
    Sc[12] = EX(Sc[12]); Sc[13] = EX(Sc[13]); SBAR();
    Oc = MFMA32(v0c, plo, Oc);
    Sc[14] = EX(Sc[14]); Sc[15] = EX(Sc[15]); pup[0] = cvtpk(Sc[8], Sc[9]); pup[1] = cvtpk(Sc[10], Sc[11]); PIN(Sc); PIN(pup); SBAR();
}
DI void att_path(lptr lds, const Params& P, int a4, int lane) {
    char* DBGWS = P.ws; (void)DBGWS;
    const int rh = a4 & 1, kh = a4 >> 1, r = lane & 31, h = lane >> 5;
    const __amdgpu_buffer_rsrc_t rs = mkrsrc(P.ws, WS_KV_END);
    const unsigned voK1 = WS_K1 + lane * 16, voK2 = h == 0 ? WS_K2 + r * 16 : OOB, voV = r < 19 ? WS_V + h * 304 + r * 16 : OOB;
    unsigned cnt_v = (unsigned)(uintptr_t)(lds + L_CNT), one_v = 1u; PIN(cnt_v); PIN(one_v);
#define LDK(B, kb) do { B.k1 = __builtin_amdgcn_raw_buffer_load_b128(rs, voK1, (kb) * 1024, 0); B.k2 = __builtin_amdgcn_raw_buffer_load_b128(rs, voK2, (kb) * 512, 0); } while (0)
#define LDV(B, kb) do { B.v0 = __builtin_amdgcn_raw_buffer_load_b128(rs, voV, (kb) * 1216, 0); B.v1 = __builtin_amdgcn_raw_buffer_load_b128(rs, voV, (kb) * 1216 + 608, 0); } while (0)
    KVf A, B;
    const int kb0 = 32 * kh;
    KVf R; LDK(R, 0);
    LDK(A, kb0); LDV(A, kb0); LDK(B, kb0 + 1);
    lds_wait_ge(lds + L_CNT + 4, 4);
    STAMP(1);
    u32x4 q1[4], q2[4];
#pragma unroll
    for (int T = 0; T < 4; ++T) { const int rt = 2 * rh + (T >> 1), a = T & 1;
        q1[T] = *(LAS u32x4*)(lds + L_Q + (rt * 2 + a) * 2048 + lane * 16); q2[T] = *(LAS u32x4*)(lds + L_Q + (rt * 2 + a) * 2048 + 1024 + lane * 16); }
    WAITL0();
#pragma unroll
    for (int T = 0; T < 4; ++T) { PIN(q1[T]); PIN(q2[T]); }
    lds_add1(cnt_v + 8, one_v);
    float mref[4];
#pragma unroll
    for (int T = 0; T < 4; ++T) {
        f32x16 s0 = MFMA32(R.k1, q1[T], ZERO16); s0 = MFMA32(R.k2, q2[T], s0);
        float m = s0[0];
#pragma unroll
        for (int i = 1; i < 16; ++i) m = fmaxf(m, s0[i]);
        m = swap32_max(m); const unsigned mb = cvtpk(-m, 0.f) & 0xffffu; mref[T] = -__uint_as_float(mb << 16);
        if (h == 0) q2[T][1] = mb;
    }
    STAMP(2);
    f32x16 O[4] = {}; f32x16 S0, S1 = {}; u32x4 pup = {0u, 0u, 0u, 0u};
    B.v1 = A.v1;
    S0 = MFMA32(A.k1, q1[0], ZERO16); S0 = MFMA32(A.k2, q2[0], S0);
    for (int it = 0; it < 16; ++it) {
        const int kb = kb0 + 2 * it;
        att_slot(S0, S1, pup, A.k1, A.k2, q1[1], q2[1], B.v1, O[3], A.v0, O[0]);
        LDV(B, kb + 1);
        att_slot(S1, S0, pup, A.k1, A.k2, q1[2], q2[2], A.v1, O[0], A.v0, O[1]);
        att_slot(S0, S1, pup, A.k1, A.k2, q1[3], q2[3], A.v1, O[1], A.v0, O[2]);
        { const int kn = (kb + 2 < 64) ? kb + 2 : 63; LDK(A, kn); }
        att_slot(S1, S0, pup, B.k1, B.k2, q1[0], q2[0], A.v1, O[2], A.v0, O[3]);
        att_slot(S0, S1, pup, B.k1, B.k2, q1[1], q2[1], A.v1, O[3], B.v0, O[0]);
        { const int kn = (kb + 2 < 64) ? kb + 2 : 63; LDV(A, kn); }
        att_slot(S1, S0, pup, B.k1, B.k2, q1[2], q2[2], B.v1, O[0], B.v0, O[1]);
        att_slot(S0, S1, pup, B.k1, B.k2, q1[3], q2[3], B.v1, O[1], B.v0, O[2]);
        { const int kn = (kb + 3 < 64) ? kb + 3 : 63; LDK(B, kn); }
        att_slot(S1, S0, pup, A.k1, A.k2, q1[0], q2[0], B.v1, O[2], B.v0, O[3]);
    }
    pup[2] = cvtpk(S1[12], S1[13]); pup[3] = cvtpk(S1[14], S1[15]);
    O[3] = MFMA32(B.v1, pup, O[3]);
    STAMP(9);
    BARRIER();
    auto send = [&](const f32x16& o, float m, int rr) {
        lptr d = lds + L_XCH + (a4 * 2 + rr) * 3072 + lane * 16;
        *(LAS f32x4*)(d) = (f32x4){o[0], o[1], o[2], o[3]}; *(LAS f32x4*)(d + 1024) = (f32x4){o[4], o[5], o[6], o[7]}; *(LAS f32x4*)(d + 2048) = (f32x4){o[8], o[9], o[10], m}; };
    if (kh) { send(O[0], mref[0], 0); send(O[2], mref[2], 1); } else { send(O[1], mref[1], 0); send(O[3], mref[3], 1); }
    BARRIER();
    auto comb = [&](const f32x16& o, float m, int rr) {
        lptr d = lds + L_XCH + ((a4 ^ 2) * 2 + rr) * 3072 + lane * 16;
        const f32x4 a0 = *(LAS f32x4*)(d), a1 = *(LAS f32x4*)(d + 1024), a2 = *(LAS f32x4*)(d + 2048);
        const float M = fmaxf(m, a2[3]), fa = EX(m - M), fb = EX(a2[3] - M);
        float v[10];
#pragma unroll
        for (int i = 0; i < 4; ++i) { v[i] = o[i] * fa + a0[i] * fb; v[4 + i] = o[4 + i] * fa + a1[i] * fb; }
        v[8] = o[8] * fa + a2[0] * fb; v[9] = o[9] * fa + a2[1] * fb;
        float l = o[10] * fa + a2[2] * fb;
        l = __shfl(l, r, 64);
        const float il = __builtin_amdgcn_rcpf(l);
        lptr od = lds + L_O + (64 * rh + 32 * rr + r) * O_STRIDE + 36 * kh;
        *(LAS unsigned*)(od + 8 * h) = cvtpk(v[0] * il, v[1] * il); *(LAS unsigned*)(od + 8 * h + 4) = cvtpk(v[2] * il, v[3] * il);
        *(LAS unsigned*)(od + 16 + 8 * h) = cvtpk(v[4] * il, v[5] * il); *(LAS unsigned*)(od + 16 + 8 * h + 4) = cvtpk(v[6] * il, v[7] * il);
        if (h == 0) *(LAS unsigned*)(od + 32) = cvtpk(v[8] * il, v[9] * il);
        else if (kh == 1) { *(LAS unsigned*)(od + 36) = 0u; *(LAS unsigned*)(od + 40) = 0u; *(LAS unsigned*)(od + 44) = 0u; *(LAS unsigned*)(od + 48) = 0u; *(LAS unsigned*)(od + 52) = 0u; *(LAS unsigned*)(od + 56) = 0u; }
    };
    if (kh) { comb(O[1], mref[1], 0); comb(O[3], mref[3], 1); } else { comb(O[0], mref[0], 0); comb(O[2], mref[2], 1); }
}

struct PhB { __amdgpu_buffer_rsrc_t rsD, rsO, rsP; unsigned voD, vo5, ldsw, so0, so4, so5, po, d5, d5str; lptr tb0; u32x4 af[3];
             lptr tc, tn; unsigned vo_a, vo5_a, dst_a, d5_a, col_a, vo_b, vo5_b, dst_b, d5_b, col_b, pst, po_p; };
template <int NH, int I> DI void phb_piece(PhB& b, unsigned dst, unsigned d5, unsigned col, unsigned vo, unsigned vo5) {
    if constexpr (I < 4) dma16(dst + I * 8192, vo, b.rsD, b.so0 + I * (64u * 1152u) + col);
    else if constexpr (I == 4) { if constexpr (NH == 0) dma16(dst + I * 8192, vo, b.rsD, b.so4 + col); else dma16(dst + I * 8192, vo, b.rsO, b.so4 + col); }
    else if constexpr (NH == 0) dma16(d5, vo5, b.rsO, b.so5 + col);
}
template <int NH, int KT> DI void phb_issue(PhB& b) {
    constexpr int o = KT >= 19 ? KT - 19 : KT + 2;
    if constexpr (o < 18 && (o % 3) == (NH ? 2 : 0)) { if constexpr (KT >= 19) phb_piece<NH, o / 3>(b, b.dst_b, b.d5_b, b.col_b, b.vo_b, b.vo5_b); else phb_piece<NH, o / 3>(b, b.dst_a, b.d5_a, b.col_a, b.vo_a, b.vo5_a); }
}
template <int NH, int KT> DI void phb_step(PhB& b, const u32x4 (&hf)[21], f32x16& acc, f32x16& prev, u32x4& w0, u32x4& w1) {
    constexpr int P = NH ? 5 : 6;
    if constexpr (KT == 19) asm volatile("s_waitcnt vmcnt(%0) lgkmcnt(0)\n\ts_barrier" :: "i"(P + 2) : "memory");
    if constexpr (KT + 2 < 21) b.af[(KT + 2) % 3] = trfrag(b.tc + (KT + 2) * 2048, 1024); else b.af[(KT + 2) % 3] = trfrag(b.tn + (KT + 2 - 21) * 2048, 1024);
    SBAR();
    if constexpr (KT == 0) acc = MFMA32(b.af[0], hf[0], ZERO16); else acc = MFMA32(b.af[KT % 3], hf[KT], acc);
    phb_issue<NH, KT>(b);
    if constexpr (KT >= 2 && KT < 6) { constexpr int i = KT - 2;
        if constexpr (i < 2) { w0[2 * i] = cvtpk(prev[4 * i], prev[4 * i + 1]); w0[2 * i + 1] = cvtpk(prev[4 * i + 2], prev[4 * i + 3]); PIN(w0); }
        else { w1[2 * i - 4] = cvtpk(prev[4 * i], prev[4 * i + 1]); w1[2 * i - 3] = cvtpk(prev[4 * i + 2], prev[4 * i + 3]); PIN(w1); }
        if constexpr (i == 1) __builtin_amdgcn_raw_buffer_store_b128(w0, b.rsP, b.po_p, b.pst, 0);
        if constexpr (i == 3) __builtin_amdgcn_raw_buffer_store_b128(w1, b.rsP, b.po_p + 1024, b.pst, 0);
    }
    SBAR();
}
template <int NH, int... Ks> DI void phb_stage_(PhB& b, const u32x4 (&hf)[21], f32x16& acc, f32x16& prev, std::integer_sequence<int, Ks...>) {
    u32x4 w0, w1; (phb_step<NH, Ks>(b, hf, acc, prev, w0, w1), ...);
}
template <int NH> DI void phb_stage(PhB& b, const u32x4 (&hf)[21], f32x16& acc, f32x16& prev, int np) {
    const unsigned b0 = (unsigned)(np % 3) * BBUF, b1 = (unsigned)((np + 1) % 3) * BBUF, b2 = (unsigned)((np + 2) % 3) * BBUF;
    b.tc = b.tb0 + b0; b.tn = b.tb0 + b1;
    b.dst_a = b.ldsw + b2; b.d5_a = b.d5 + ((np + 2) % 3) * b.d5str; b.col_a = (np + 2) * 128u; b.vo_a = np + 2 < 9 ? b.voD : OOB; b.vo5_a = np + 2 < 9 ? b.vo5 : OOB;
    b.dst_b = b.ldsw + b0; b.d5_b = b.d5 + (np % 3) * b.d5str; b.col_b = (np + 3) * 128u; b.vo_b = np + 3 < 9 ? b.voD : OOB; b.vo5_b = np + 3 < 9 ? b.vo5 : OOB;
    b.pst = np > 0 ? (unsigned)(np - 1) * 4096u : 0u; b.po_p = np > 0 ? b.po : OOB;
    phb_stage_<NH>(b, hf, acc, prev, std::make_integer_sequence<int, 21>{});
}
template <int NH> DI void phb_run(PhB& b, const u32x4 (&hf)[21], char* DBGWS) {
    b.af[0] = trfrag(b.tb0, 1024); b.af[1] = trfrag(b.tb0 + 2048, 1024);
    phb_piece<NH, 0>(b, b.ldsw + BBUF, b.d5 + b.d5str, 128u, b.voD, b.vo5); phb_piece<NH, 1>(b, b.ldsw + BBUF, b.d5 + b.d5str, 128u, b.voD, b.vo5); phb_piece<NH, 2>(b, b.ldsw + BBUF, b.d5 + b.d5str, 128u, b.voD, b.vo5);
    phb_piece<NH, 3>(b, b.ldsw + BBUF, b.d5 + b.d5str, 128u, b.voD, b.vo5); phb_piece<NH, 4>(b, b.ldsw + BBUF, b.d5 + b.d5str, 128u, b.voD, b.vo5); phb_piece<NH, 5>(b, b.ldsw + BBUF, b.d5 + b.d5str, 128u, b.voD, b.vo5);
    if constexpr (NH == 0) phb_piece<NH, 0>(b, b.ldsw + 2 * BBUF, b.d5 + 2 * b.d5str, 256u, b.voD, b.vo5);
    f32x16 a0 = {}, a1 = {};
    for (int np = 0; np < 9; np += 2) {
        phb_stage<NH>(b, hf, a0, a1, np);
        STAMP(13 + np);
        if (np + 1 < 9) phb_stage<NH>(b, hf, a1, a0, np + 1);
        STAMP(14 + np);
    }
    u32x4 w0, w1;
#pragma unroll
    for (int i = 0; i < 4; ++i) { w0[i] = cvtpk(a0[2 * i], a0[2 * i + 1]); w1[i] = cvtpk(a0[8 + 2 * i], a0[9 + 2 * i]); }
    __builtin_amdgcn_raw_buffer_store_b128(w0, b.rsP, b.po, 8 * 4096, 0); __builtin_amdgcn_raw_buffer_store_b128(w1, b.rsP, b.po + 1024, 8 * 4096, 0);
}

__global__ __launch_bounds__(512, 2) void main_kernel(Params P) {
    extern __shared__ __attribute__((aligned(16))) char smem[];
    lptr lds = (lptr)smem;
    const int tid = threadIdx.x, lane = tid & 63, wid = __builtin_amdgcn_readfirstlane(tid >> 6);
    const int bid = blockIdx.x;
    const int s = 2 * (bid & 7) + ((bid >> 3) & 1), mt = bid >> 4;
    char* DBGWS = P.ws; (void)DBGWS;
    STAMPR(30); STAMP(0);
    if (tid < 16) *(LAS unsigned*)(lds + L_CNT + 4 * tid) = 0u;
    BARRIER();
    if (wid >= 4) { ffn_path(lds, P, mt, s, wid & 3, lane); BARRIER(); BARRIER(); }
    else att_path(lds, P, wid & 3, lane);
    BARRIER();
    STAMP(23);
    const int rt = wid & 3, nh = wid >> 2, r = lane & 31, h = lane >> 5, g4 = lane >> 4, q = (lane & 15) >> 2, p = lane & 3;
    u32x4 hf[21];
#pragma unroll
    for (int t = 0; t < 18; ++t) hf[t] = *(LAS u32x4*)(lds + L_H + ((t >> 1) * 4 + rt) * 2048 + (t & 1) * 1024 + lane * 16);
#pragma unroll
    for (int t = 0; t < 3; ++t) { lptr o = lds + L_O + (32 * rt + r) * O_STRIDE + 32 * t + 8 * h; const u32x2 a = *(LAS u32x2*)o, b = *(LAS u32x2*)(o + 16); hf[18 + t] = (u32x4){a[0], a[1], b[0], b[1]}; }
    PhB b;
    b.rsD = mkrsrc(P.wd, FF * DM * 2); b.rsO = mkrsrc(P.wo, DM * DM * 2);
    b.voD = (lane >> 3) * 1152 + (((lane & 7) ^ (((lane >> 4) & 1) << 2)) * 16); b.vo5 = wid < 2 ? b.voD : OOB;
    b.ldsw = (unsigned)(uintptr_t)(lds + L_RING) + wid * 1024;
    b.d5 = wid < 2 ? b.ldsw + 5 * 8192 : (unsigned)(uintptr_t)(lds + L_RING) + 3 * BBUF; b.d5str = wid < 2 ? BBUF : 0u;
    b.so0 = (unsigned)(288 * s + 8 * wid) * 1152u;
    b.so4 = wid < 4 ? (unsigned)(288 * s + 8 * (wid + 32)) * 1152u : (unsigned)(36 * s + 8 * (wid - 4)) * 1152u;
    b.so5 = (unsigned)(36 * s + 8 * (wid + 4)) * 1152u;
    const unsigned rowl = 4 * h + q; const unsigned ch = (4 * nh + 2 * (g4 & 1) + (p >> 1)) ^ (((rowl >> 1) & 1) << 2);
    b.tb0 = lds + L_RING + rowl * 128 + ch * 16 + 8 * (p & 1);
    b.rsP = mkrsrc(P.ws + WS_PART + (size_t)(mt * 16 + s) * (4 * 18 * 2048), 4 * 18 * 2048);
    b.po = (unsigned)(rt * 18 + nh) * 2048u + lane * 16;
    if (nh) phb_piece<1, 4>(b, b.ldsw, b.d5, 0u, b.voD, b.vo5); else if (wid < 2) phb_piece<0, 5>(b, b.ldsw, b.d5, 0u, b.voD, b.vo5);
    WAITL0();
#pragma unroll
    for (int t = 0; t < 21; ++t) PIN(hf[t]);
    WAITV(0);
    BARRIER();
    STAMP(24);
    if (nh) phb_run<1>(b, hf, DBGWS); else phb_run<0>(b, hf, DBGWS);
    STAMP(25); STAMPR(31);
}

__global__ __launch_bounds__(256) void final_kernel(const u16* __restrict__ x, const char* __restrict__ ws, u16* __restrict__ out) {
    const int lane = threadIdx.x & 63, unit = blockIdx.x * 4 + (threadIdx.x >> 6);
    const int R = unit / 18, nt = unit - 18 * R, mt = R >> 2, rt = R & 3, r = lane & 31, h = lane >> 5;
    const char* pb = ws + WS_PART + (size_t)mt * 16 * (4 * 18 * 2048) + (size_t)(rt * 18 + nt) * 2048 + lane * 16;
    u32x4 a[16], b[16];
#pragma unroll
    for (int s = 0; s < 16; ++s) { a[s] = *(const u32x4*)(pb + (size_t)s * (4 * 18 * 2048)); b[s] = *(const u32x4*)(pb + (size_t)s * (4 * 18 * 2048) + 1024); }
    const size_t ro = (size_t)(32 * R + r) * DM + 32 * nt + 4 * h;
    u32x2 xv[4];
#pragma unroll
    for (int i = 0; i < 4; ++i) xv[i] = *(const u32x2*)(x + ro + 8 * i);
    float acc[16];
#pragma unroll
    for (int i = 0; i < 16; ++i) acc[i] = 0.f;
#pragma unroll
    for (int s = 0; s < 16; ++s)
#pragma unroll
        for (int i = 0; i < 4; ++i) { acc[2 * i] += blo(a[s][i]); acc[2 * i + 1] += bhi(a[s][i]); acc[8 + 2 * i] += blo(b[s][i]); acc[9 + 2 * i] += bhi(b[s][i]); }
#pragma unroll
    for (int i = 0; i < 4; ++i) {
        const unsigned o0 = cvtpk(acc[4 * i] + blo(xv[i][0]), acc[4 * i + 1] + bhi(xv[i][0])), o1 = cvtpk(acc[4 * i + 2] + blo(xv[i][1]), acc[4 * i + 3] + bhi(xv[i][1]));
        *(u32x2*)(out + ro + 8 * i) = (u32x2){o0, o1};
    }
}

extern "C" void kernel_launch(void* const* d_in, const int* in_sizes, int n_in, void* d_out, int out_size, void* d_ws, size_t ws_size, hipStream_t stream) {
    const u16* x = (const u16*)d_in[0];
    Params P; P.x = x; P.wq = (const u16*)d_in[1]; P.wo = (const u16*)d_in[4]; P.wg = (const u16*)d_in[6]; P.wu = (const u16*)d_in[7]; P.wd = (const u16*)d_in[8]; P.ws = (char*)d_ws;
    (void)hipFuncSetAttribute((const void*)main_kernel, hipFuncAttributeMaxDynamicSharedMemorySize, LDS_BYTES);
    (void)hipFuncSetAttribute((const void*)kv_kernel, hipFuncAttributeMaxDynamicSharedMemorySize, KV_LDS);
    kv_kernel<<<256, 256, KV_LDS, stream>>>(x, (const u16*)d_in[2], (const u16*)d_in[3], (char*)d_ws);
    main_kernel<<<256, 512, LDS_BYTES, stream>>>(P);
    final_kernel<<<288, 256, 0, stream>>>(x, (const char*)d_ws, (u16*)d_out);
}
```

```cpp
#include <hip/hip_runtime.h>
#include <stdint.h>
#include <utility>

#define XMAP 4
#define LDAUX 16
#define POLL_SLEEP 8
#define PF_AT 3
#define KV_SLEEP 8
#define FFN_PRIO 1
#define ATT_TOGGLE 2
#define KV_PRIO 3
#define DI __device__ __forceinline__
#define LAS __attribute__((address_space(3)))
#define GAS __attribute__((address_space(1)))
typedef unsigned short u16;
typedef unsigned long long u64;
typedef __bf16 bf16x8 __attribute__((ext_vector_type(8)));
typedef __bf16 bf16x2 __attribute__((ext_vector_type(2)));
typedef short s16x4 __attribute__((ext_vector_type(4)));
typedef float f32x2 __attribute__((ext_vector_type(2)));
typedef float f32x4 __attribute__((ext_vector_type(4)));
typedef float f32x16 __attribute__((ext_vector_type(16)));
typedef unsigned u32x2 __attribute__((ext_vector_type(2)));
typedef unsigned u32x4 __attribute__((ext_vector_type(4)));
typedef LAS char* lptr;

constexpr int SEQ = 2048, DM = 576, FF = 4608;
constexpr unsigned WS_K1 = 0, WS_K2 = 65536, WS_V = WS_K2 + 64 * 512, WS_KV_END = WS_V + 128 * 608;
constexpr unsigned WS_XN = 262144, XN_BYTES = SEQ * DM * 2;
constexpr unsigned WS_G = 3u << 20, WS_FA = WS_G + 4096, WS_FB = WS_G + 8192, WS_FC = WS_FB + 16384;
constexpr size_t WS_PART = 4u << 20;
constexpr unsigned SLAB = 4 * 18 * 2048;
constexpr unsigned SLOT = 12288, L_CNT = 0, L_RING = 4096, L_H = L_RING + 6 * SLOT, L_Q = L_H + 57344, L_O = L_H + 73728, O_STRIDE = 96, L_XCH = L_RING + 4 * SLOT, LDS_BYTES = 163840;
constexpr unsigned BBUF = 43008;
constexpr unsigned L_X = 16384, L_KVX = L_H + 43008, KV_XS = 1168, KV_LDS = 43008 + 8 * 1168;
static_assert(L_O + 128 * O_STRIDE <= LDS_BYTES && L_RING + 3 * BBUF + 1024 <= L_O && KV_LDS <= 57344 && L_X + 128 * 1152 <= LDS_BYTES , "LDS map");
constexpr unsigned C_RING = 0, C_Q = 4, C_QTAKEN = 8, C_KV1 = 12, C_KV2 = 16, C_KVOK = 20, C_ATTP = 32, C_MASK = 48;
constexpr unsigned OOB = 0x40000000u;
constexpr int SC1 = 16;
constexpr float QSCALE = 0.23570226039551584f * 1.4426950408889634f;

#define SBAR() __builtin_amdgcn_sched_barrier(0)
#define PIN(x) asm volatile("" : "+v"(x))
#define MFMA32(a, b, c) __builtin_amdgcn_mfma_f32_32x32x16_bf16(__builtin_bit_cast(bf16x8, a), __builtin_bit_cast(bf16x8, b), c, 0, 0, 0)
#define MFMA16(a, b, c) __builtin_amdgcn_mfma_f32_16x16x32_bf16(__builtin_bit_cast(bf16x8, a), __builtin_bit_cast(bf16x8, b), c, 0, 0, 0)
#define WAITV(n) asm volatile("s_waitcnt vmcnt(" #n ")" ::: "memory")
#define WAITL0() asm volatile("s_waitcnt lgkmcnt(0)" ::: "memory")
#define FENCE() asm volatile("" ::: "memory")
#define BARRIER() asm volatile("s_waitcnt lgkmcnt(0)\n\ts_barrier" ::: "memory")
#define ZERO16 ((f32x16){})
#define RLX_AGENT __ATOMIC_RELAXED, __HIP_MEMORY_SCOPE_AGENT

DI unsigned cvtpk(float lo, float hi) { f32x2 v = {lo, hi}; return __builtin_bit_cast(unsigned, __builtin_convertvector(v, bf16x2)); }
DI float bfr(float f) { return __uint_as_float(cvtpk(f, 0.f) << 16); }
DI float blo(unsigned v) { return __uint_as_float(v << 16); }
DI float bhi(unsigned v) { return __uint_as_float(v & 0xffff0000u); }
DI u32x2 trread(lptr p) { return __builtin_bit_cast(u32x2, __builtin_amdgcn_ds_read_tr16_b64_v4i16((LAS s16x4*)p)); }
DI u32x4 trfrag(lptr p, unsigned second) { u32x2 a = trread(p), b = trread(p + second); return (u32x4){a[0], a[1], b[0], b[1]}; }
DI __amdgpu_buffer_rsrc_t mkrsrc(const void* p, unsigned bytes) { return __builtin_amdgcn_make_buffer_rsrc((void*)p, (short)0, (int)bytes, 0x00020000); }
DI void dma16(unsigned ldsaddr, unsigned voff, __amdgpu_buffer_rsrc_t rs, unsigned soff) {
    unsigned keep;
    asm volatile("s_mov_b32 %0, m0\n\ts_mov_b32 m0, %1\n\ts_nop 0\n\tbuffer_load_dwordx4 %2, %3, %4 offen lds\n\ts_mov_b32 m0, %0"
                 : "=&s"(keep) : "s"(ldsaddr), "v"(voff), "s"(rs), "s"(soff) : "memory");
}
DI float swap32_max(float v) { auto r = __builtin_amdgcn_permlane32_swap(__float_as_uint(v), __float_as_uint(v), false, false); return fmaxf(__uint_as_float(r[0]), __uint_as_float(r[1])); }
DI void lds_add1(unsigned addr_v, unsigned one_v) { asm volatile("s_mov_b64 exec, 1\n\tds_add_u32 %0, %1\n\ts_mov_b64 exec, -1" :: "v"(addr_v), "v"(one_v) : "memory"); }
DI unsigned lds_ld(lptr p) { return __hip_atomic_load((LAS unsigned*)p, __ATOMIC_RELAXED, __HIP_MEMORY_SCOPE_WORKGROUP); }
DI void lds_wait_ge(lptr p, unsigned v) { for (unsigned n = 0; (unsigned)__builtin_amdgcn_readfirstlane(lds_ld(p)) < v && n < (1u << 22); ++n) __builtin_amdgcn_s_sleep(1); FENCE(); }
DI u64 gld64(const char* p) { return __hip_atomic_load((GAS u64*)p, RLX_AGENT); }
DI void gst64(char* p, u64 v) { __hip_atomic_store((GAS u64*)p, v, RLX_AGENT); }
template <int PER_LANE, int SLEEP = POLL_SLEEP> DI void wait_flags(const char* base, int nlanes, u64 tag, int lane) {
    for (unsigned n = 0; n < (1u << 18); ++n) { bool ok = true;
        if (lane < nlanes) {
#pragma unroll
            for (int i = 0; i < PER_LANE; ++i) ok &= gld64(base + (lane + 64 * i) * 8) == tag; }
        if (__all(ok)) break;
        __builtin_amdgcn_s_sleep(SLEEP); }
    FENCE();
}
DI void acquire_agent() { __builtin_amdgcn_fence(__ATOMIC_ACQUIRE, "agent"); WAITV(0); }

#define STAMP(i) do {} while (0)
#define STAMPR(i) do {} while (0)

struct Params { const u16* x; const u16* wq; const u16* wk; const u16* wv; const u16* wo; const u16* wg; const u16* wu; const u16* wd; u16* out; char* ws; };

template <int CTRL> DI float dpp_add(float v) { return v + __uint_as_float(__builtin_amdgcn_update_dpp(0u, __float_as_uint(v), CTRL, 0xf, 0xf, true)); }
DI float sum32(float v) {
    v = dpp_add<0xB1>(v); v = dpp_add<0x4E>(v); v = dpp_add<0x141>(v); v = dpp_add<0x140>(v);
    auto r = __builtin_amdgcn_permlane16_swap(__float_as_uint(v), __float_as_uint(v), false, false); return __uint_as_float(r[0]) + __uint_as_float(r[1]);
}
constexpr unsigned KV_WV = 21 * 1024;
struct KvPre { u32x4 w[11]; };
DI void kv_preload(KvPre& k, int t256, const Params& P) {
    const __amdgpu_buffer_rsrc_t rk = mkrsrc(P.wk, DM * 18 * 2), rv = mkrsrc(P.wv, DM * 18 * 2);
#pragma unroll
    for (int i = 0; i < 11; ++i) { const int id = t256 + 256 * i;
        if (i < 5) k.w[i] = __builtin_amdgcn_raw_buffer_load_b128(rk, id * 16, 0, 0);
        else if (i == 5) k.w[i] = id < 1296 ? __builtin_amdgcn_raw_buffer_load_b128(rk, id * 16, 0, 0) : __builtin_amdgcn_raw_buffer_load_b128(rv, (id - 1296) * 16, 0, 0);
        else k.w[i] = __builtin_amdgcn_raw_buffer_load_b128(rv, (id - 1296) * 16, 0, 0); }
}
DI void kv_job(lptr lds, const KvPre& k, int a, int lane, int mt, int s, const Params& P, u64 tag, unsigned cnt_v, unsigned one_v, int bid) {
    char* ws = P.ws; char* DBGWS = ws; (void)DBGWS;
    lptr cnt = lds + L_CNT;
    __builtin_amdgcn_s_setprio(KV_PRIO);
    const int hr = lane >> 5, c = lane & 31, rr = 2 * a + hr, lrow = 8 * s + rr;
    unsigned xv[9];
#pragma unroll
    for (int j = 0; j < 9; ++j) { const unsigned dw = c + 32 * j, ch = dw >> 2; xv[j] = *(LAS unsigned*)(lds + L_X + lrow * 1152 + (((ch & ~7u) | ((ch & 7u) ^ ((lrow >> 1) & 7u))) << 4) + 4 * (dw & 3)); }
    float sum = 0.f;
#pragma unroll
    for (int j = 0; j < 9; ++j) sum += blo(xv[j]) + bhi(xv[j]);
    const float mean = sum32(sum) * (1.f / DM);
    float sq = 0.f;
#pragma unroll
    for (int j = 0; j < 9; ++j) { const float d0 = blo(xv[j]) - mean, d1 = bhi(xv[j]) - mean; sq = __builtin_fmaf(d0, d0, sq); sq = __builtin_fmaf(d1, d1, sq); }
    const float mb = bfr(mean), sd = bfr(__builtin_amdgcn_sqrtf(bfr(bfr(sum32(sq) * (1.f / DM)) + 1e-5f))), rinv = __builtin_amdgcn_rcpf(sd);
#pragma unroll
    for (int j = 0; j < 9; ++j) xv[j] = cvtpk(bfr(blo(xv[j]) - mb) * rinv, bfr(bhi(xv[j]) - mb) * rinv);
    STAMP(33);
    BARRIER();
#pragma unroll
    for (int j = 0; j < 9; ++j) *(LAS unsigned*)(lds + L_KVX + rr * KV_XS + 4 * (c + 32 * j)) = xv[j];
    { const int t256 = a * 64 + lane;
#pragma unroll
      for (int i = 0; i < 11; ++i) { const int id = t256 + 256 * i; if (id < 2592) *(LAS u32x4*)(lds + L_H + (id < 1296 ? id * 16 : KV_WV + (id - 1296) * 16)) = k.w[i]; } }
    WAITL0();
    lds_add1(cnt_v + C_KV1, one_v);
    lds_wait_ge(cnt + C_KV1, 4);
    STAMP(36);
    const int i16 = lane & 15, g = lane >> 4;
    const int col = (a & 1) ? (16 + (i16 < 2 ? i16 : 1)) : i16;
    lptr wp = lds + L_H + (a < 2 ? 0u : KV_WV) + (8 * g) * 36 + 2 * col;
    lptr xp = lds + L_KVX + (i16 & 7) * KV_XS + 16 * g;
    f32x4 acc = {0.f, 0.f, 0.f, 0.f};
    {
        u32x4 wf[18], xq[18];
#pragma unroll
        for (int t = 0; t < 18; ++t) {
#pragma unroll
            for (int j = 0; j < 4; ++j) wf[t][j] = (unsigned)*(LAS u16*)(wp + t * 32 * 36 + (2 * j) * 36) | ((unsigned)*(LAS u16*)(wp + t * 32 * 36 + (2 * j + 1) * 36) << 16);
            xq[t] = *(LAS u32x4*)(xp + t * 64); }
#pragma unroll
        for (int t = 0; t < 18; ++t) { PIN(wf[t]); PIN(xq[t]); }
        if (a < 2) {
#pragma unroll
            for (int t = 0; t < 18; ++t) acc = MFMA16(wf[t], xq[t], acc);
        } else {
#pragma unroll
            for (int t = 0; t < 18; ++t) acc = MFMA16(xq[t], wf[t], acc);
        }
    }
    const __amdgpu_buffer_rsrc_t rs = mkrsrc(ws, WS_KV_END);
    const int ky0 = 128 * mt + 8 * s;
    const unsigned p01 = cvtpk(acc[0], acc[1]), p23 = cvtpk(acc[2], acc[3]);
    if (a == 0) { if (i16 < 8) { const int ky = ky0 + i16; __builtin_amdgcn_raw_buffer_store_b64((u32x2){p01, p23}, rs, WS_K1 + (ky >> 5) * 1024 + ((ky & 31) + 32 * (g & 1)) * 16 + 8 * (g >> 1), 0, SC1); } }
    else if (a == 1) { if (i16 < 8 && g == 0) { const int ky = ky0 + i16; __builtin_amdgcn_raw_buffer_store_b128((u32x4){p01, 0x00003F80u, 0u, 0u}, rs, WS_K2 + (ky >> 5) * 512 + (ky & 31) * 16, 0, SC1); } }
    else { const int cc = (a == 2 ? 0 : 16) + i16; const bool ones = (cc == 18);
        if (g < 2 && cc <= 18) __builtin_amdgcn_raw_buffer_store_b64(ones ? (u32x2){0x3F803F80u, 0x3F803F80u} : (u32x2){p01, p23}, rs, WS_V + (ky0 >> 4) * 608 + g * 304 + cc * 16 + 8 * (s & 1), 0, SC1); }
    __builtin_amdgcn_s_setprio(0);
    STAMP(37);
    WAITV(0);
    STAMP(38);
    lds_add1(cnt_v + C_KV2, one_v);
    if (a == 0) { lds_wait_ge(cnt + C_KV2, 4);
        if (lane < 8) gst64(ws + WS_FB + lane * 2048 + bid * 8, tag); }
}

struct Tgt { __amdgpu_buffer_rsrc_t rs[3]; unsigned base[3], ld[3], jstr; unsigned vo; };
struct Ffn {
    u32x4 xf[36];
    u32x4 ag[3], au[3];
    lptr rb0, rb1;
    Tgt cur, nxt;
    lptr lds; unsigned sync_target; unsigned cnt_v, one_v;
};
template <int Q> DI void ffn_dma(const Tgt& t, int j) { dma16(t.ld[Q] + j * SLOT, t.vo, t.rs[Q], t.base[Q] + j * t.jstr); }
template <int T, int U> DI void ffn_read1(Ffn& f) {
    constexpr int j = T / 6, kk = T % 6; lptr b = (j < 3 ? f.rb0 : f.rb1) + (j % 3) * SLOT + kk * 1024 + U * 6144;
    if constexpr (U) f.au[T % 3] = trfrag(b, 256); else f.ag[T % 3] = trfrag(b, 256);
}
template <int T> DI void ffn_issue(Ffn& f) {
    constexpr int j = T / 6, kk = T % 6;
    if constexpr (kk == 4 || kk == 0 || kk == 2) {
        constexpr int q = kk == 4 ? 0 : kk == 0 ? 1 : 2; constexpr int m = (kk == 4 ? j : j - 1) + 5;
        if constexpr (m < 6) ffn_dma<q>(f.cur, m); else ffn_dma<q>(f.nxt, m - 6);
    }
}
#define SYNC_ON true
template <int T> DI void ffn_norm(Ffn& f, f32x2 r2, f32x2 c2) {
    float lo[4], hi[4];
#pragma unroll
    for (int i = 0; i < 4; ++i) { lo[i] = blo(f.xf[T][i]); hi[i] = bhi(f.xf[T][i]); }
    SBAR();
    f32x2 t[4];
#pragma unroll
    for (int i = 0; i < 4; ++i) t[i] = __builtin_elementwise_fma((f32x2){lo[i], hi[i]}, r2, c2);
    SBAR();
#pragma unroll
    for (int i = 0; i < 4; ++i) f.xf[T][i] = cvtpk(t[i][0], t[i][1]);
    PIN(f.xf[T]);
}
template <bool FIRST, int T> DI void ffn_step(Ffn& f, f32x16& g, f32x16& u, unsigned& polled, f32x2 r2, f32x2 c2) {
    constexpr int kk = T % 6;
    if constexpr (SYNC_ON && kk == 1) {
        WAITV(8);
        lds_add1(f.cnt_v + C_RING, f.one_v);
        f.sync_target += 4;
    }
    if constexpr (SYNC_ON && kk == 3) polled = lds_ld(f.lds + L_CNT + C_RING);
    if constexpr (SYNC_ON && kk == 4) {
        if (__builtin_expect((unsigned)__builtin_amdgcn_readfirstlane(polled) < f.sync_target, 0)) {
            for (unsigned n = 0; (unsigned)__builtin_amdgcn_readfirstlane(lds_ld(f.lds + L_CNT + C_RING)) < f.sync_target && n < (1u << 22); ++n) {} }
        FENCE();
    }
    ffn_read1<(T + 2) % 36, 0>(f);
    SBAR();
    if constexpr (T == 0) g = MFMA32(f.ag[0], f.xf[0], ZERO16); else g = MFMA32(f.ag[T % 3], f.xf[T], g);
    ffn_issue<T>(f);
    SBAR();
    ffn_read1<(T + 2) % 36, 1>(f);
    SBAR();
    if constexpr (T == 0) u = MFMA32(f.au[0], f.xf[0], ZERO16); else u = MFMA32(f.au[T % 3], f.xf[T], u);
    if constexpr (FIRST && T + 2 < 36) ffn_norm<T + 2>(f, r2, c2);
    SBAR();
}
template <bool FIRST, int... Ts> DI void ffn_chunk_(Ffn& f, f32x16& g, f32x16& u, f32x2 r2, f32x2 c2, std::integer_sequence<int, Ts...>) { unsigned polled = 0; (ffn_step<FIRST, Ts>(f, g, u, polled, r2, c2), ...); }

DI void ffn_path(lptr lds, const Params& P, int mt, int s, int w, int lane) {
    char* DBGWS = P.ws; (void)DBGWS;
    Ffn f; f.lds = lds;
    const int h = lane >> 5, g4 = lane >> 4, q = (lane & 15) >> 2, p = lane & 3;
    const unsigned ring = (unsigned)(uintptr_t)(lds + L_RING);
    const __amdgpu_buffer_rsrc_t rsG = mkrsrc(P.wg, DM * FF * 2), rsU = mkrsrc(P.wu, DM * FF * 2), rs1 = mkrsrc(w < 2 ? P.wg : P.wu, DM * FF * 2);
    { const __amdgpu_buffer_rsrc_t rsQ = mkrsrc(P.wq, DM * DM * 2);
#pragma unroll
      for (int i = 0; i < 3; ++i) { const int pi = w + 4 * i; const bool up = pi >= 6; const int pr = up ? pi - 6 : pi; const unsigned row = 16u * pr;
          f.cur.ld[i] = ring + (up ? 6144u : 0u) + pr * 1024u; f.cur.rs[i] = rsQ; f.cur.base[i] = row * 1152u + (unsigned)s * 72u + (up ? 36u : 0u);
          f.nxt.ld[i] = f.cur.ld[i]; f.nxt.base[i] = row * 9216u + (unsigned)s * 576u; }
      f.cur.jstr = 96u * 1152u; f.cur.vo = (lane >> 2) * 1152 + (lane & 3) * 16;
      f.nxt.rs[0] = rsG; f.nxt.rs[1] = rs1; f.nxt.rs[2] = rsU; f.nxt.jstr = 96u * 9216u; f.nxt.vo = (lane >> 2) * 9216 + (lane & 3) * 16; }
    f.rb0 = lds + L_RING + (8 * h + q) * 64 + (g4 & 1) * 32 + 8 * p; { lptr t1 = f.rb0 + 3 * SLOT; PIN(t1); f.rb1 = t1; }
    { unsigned cv = (unsigned)(uintptr_t)(lds + L_CNT), ov = 1u; PIN(cv); PIN(ov); f.cnt_v = cv; f.one_v = ov; }
    f32x16 sA = {}, sB = {}; f32x2 r2, c2;
    {
        const int r = lane & 31; const unsigned gx = (r >> 1) & 7; lptr xb = lds + L_X + (32 * w + r) * 1152;
        lptr xb4[4];
#pragma unroll
        for (int i = 0; i < 4; ++i) xb4[i] = xb + 16 * ((2 * i + h) ^ gx);
#pragma unroll
        for (int t = 0; t < 36; ++t) f.xf[t] = *(LAS u32x4*)(xb4[t & 3] + 128 * (t >> 2));
    }
    const u32x4 ones = {0x3F803F80u, 0x3F803F80u, 0x3F803F80u, 0x3F803F80u};
#pragma unroll
    for (int t = 0; t < 16; ++t) { sA = MFMA32(ones, f.xf[t], sA); sB = MFMA32(f.xf[t], f.xf[t], sB); PIN(sA); PIN(sB); SBAR(); }
    BARRIER();
    STAMP(26);
#define STAT(t) do { sA = MFMA32(ones, f.xf[t], sA); sB = MFMA32(f.xf[t], f.xf[t], sB); PIN(sA); PIN(sB); SBAR(); } while (0)
    STAT(16); STAT(17); STAT(18);
    ffn_dma<0>(f.cur, 1); STAT(19); ffn_dma<1>(f.cur, 1); STAT(20); ffn_dma<2>(f.cur, 1); STAT(21);
    ffn_dma<0>(f.cur, 2); STAT(22); ffn_dma<1>(f.cur, 2); STAT(23); ffn_dma<2>(f.cur, 2); STAT(24);
    ffn_dma<0>(f.cur, 3); STAT(25); ffn_dma<1>(f.cur, 3); STAT(26); ffn_dma<2>(f.cur, 3); STAT(27);
    ffn_dma<0>(f.cur, 4); STAT(28); STAT(29); STAT(30); STAT(31); STAT(32); STAT(33); STAT(34); STAT(35);
#undef STAT
    {
        const float rowsum = sA[0];
        const int r = lane & 31, reg = (r & 3) + 4 * (r >> 3);
        float dg = sB[0];
#pragma unroll
        for (int i = 1; i < 16; ++i) dg = reg == i ? sB[i] : dg;
        dg = ((r >> 2) & 1) == h ? dg : 0.f;
        auto sw = __builtin_amdgcn_permlane32_swap(__float_as_uint(dg), __float_as_uint(dg), false, false);
        const float sq = __uint_as_float(sw[0]) + __uint_as_float(sw[1]);
        const float mean = rowsum * (1.f / DM); float var = fmaxf(sq * (1.f / DM) - mean * mean, 0.f);
        if (__builtin_expect(__any(mean * mean > 256.f * var), 0)) {
            float v2 = 0.f;
#pragma unroll
            for (int t = 0; t < 36; ++t) { PIN(f.xf[t]); PIN(v2);
#pragma unroll
                for (int i = 0; i < 4; ++i) { const float d0 = blo(f.xf[t][i]) - mean, d1 = bhi(f.xf[t][i]) - mean; v2 = __builtin_fmaf(d0, d0, v2); v2 = __builtin_fmaf(d1, d1, v2); } }
            auto s2 = __builtin_amdgcn_permlane32_swap(__float_as_uint(v2), __float_as_uint(v2), false, false);
            var = (__uint_as_float(s2[0]) + __uint_as_float(s2[1])) * (1.f / DM);
        }
        const float mb = bfr(mean), sd = bfr(__builtin_amdgcn_sqrtf(bfr(bfr(var) + 1e-5f)));
        const float rinv = __builtin_amdgcn_rcpf(sd), cc = -mb * rinv; r2 = (f32x2){rinv, rinv}; c2 = (f32x2){cc, cc};
    }
    PIN(r2); PIN(c2);
#pragma unroll
    for (int t = 0; t < 36; ++t) PIN(f.xf[t]);
    ffn_norm<0>(f, r2, c2); ffn_norm<1>(f, r2, c2);
    STAMP(27);
    f.sync_target = 0;
    STAMP(2);
    ffn_read1<0, 0>(f); ffn_read1<0, 1>(f); ffn_read1<1, 0>(f); ffn_read1<1, 1>(f);
    __builtin_amdgcn_s_setprio(FFN_PRIO);
    auto next_params = [&](int c) {
        f.cur = f.nxt;
        if (c < 8) {
#pragma unroll
            for (int i = 0; i < 3; ++i) f.nxt.base[i] += 64u;
        } else if (c == 8) {
            const __amdgpu_buffer_rsrc_t rsD = mkrsrc(P.wd, FF * DM * 2);
#pragma unroll
            for (int i = 0; i < 3; ++i) { f.nxt.rs[i] = rsD; f.nxt.ld[i] = ring + 1024u * (w + 4 * i); f.nxt.base[i] = (288u * s + 8u * (w + 4 * i)) * 1152u; }
            f.nxt.jstr = 96u * 1152u; f.nxt.vo = (lane >> 3) * 1152 + (((lane & 7) ^ (((lane >> 4) & 1) << 2)) * 16);
        }
    };
    {
        f32x16 g, u;
        ffn_chunk_<true>(f, g, u, r2, c2, std::make_integer_sequence<int, 36>{});
        const bool lo = lane < 32;
#pragma unroll
        for (int a = 0; a < 2; ++a) { const f32x16& qq = a ? u : g; u32x4 f0, f1;
#pragma unroll
            for (int i = 0; i < 4; ++i) f0[i] = cvtpk(qq[2 * i] * QSCALE, qq[2 * i + 1] * QSCALE);
            f1 = (u32x4){lo ? cvtpk(qq[8] * QSCALE, qq[9] * QSCALE) : 0u, 0u, 0u, 0u};
            *(LAS u32x4*)(lds + L_Q + (w * 2 + a) * 2048 + lane * 16) = f0;
            *(LAS u32x4*)(lds + L_Q + (w * 2 + a) * 2048 + 1024 + lane * 16) = f1; }
        WAITL0();
        lds_add1(f.cnt_v + C_Q, f.one_v);
        next_params(0);
        STAMP(3);
    }
    lptr hw = lds + L_H + w * 2048 + lane * 16;
    for (int c = 1; c < 10; ++c) {
        f32x16 g, u;
        ffn_chunk_<false>(f, g, u, r2, c2, std::make_integer_sequence<int, 36>{});
        if (c == 1) lds_wait_ge(lds + L_CNT + C_KV2, 4);
        if (c == 8) lds_wait_ge(lds + L_CNT + C_QTAKEN, 4);
        u32x4 w0, w1;
#pragma unroll
        for (int i = 0; i < 4; ++i) { w0[i] = cvtpk(__builtin_fmaxf(g[2 * i], 0.f) * u[2 * i], __builtin_fmaxf(g[2 * i + 1], 0.f) * u[2 * i + 1]);
                                      w1[i] = cvtpk(__builtin_fmaxf(g[8 + 2 * i], 0.f) * u[8 + 2 * i], __builtin_fmaxf(g[9 + 2 * i], 0.f) * u[9 + 2 * i]); }
        *(LAS u32x4*)(hw + (c - 1) * 8192) = w0; *(LAS u32x4*)(hw + (c - 1) * 8192 + 1024) = w1;
        next_params(c);
        STAMP(3 + c);
    }
    __builtin_amdgcn_s_setprio(0);
    WAITV(0);
}

struct KVf { u32x4 k1, k2, v0, v1; };
#define EX(x) __builtin_amdgcn_exp2f(x)
#define APRIO_HI() __builtin_amdgcn_s_setprio(ATT_TOGGLE)
#define APRIO_LO() __builtin_amdgcn_s_setprio(0)
DI void att_slot(f32x16& Sc, f32x16& Sn, u32x4& pup, const u32x4& k1, const u32x4& k2, const u32x4& q1, const u32x4& q2,
                 const u32x4& v1p, f32x16& Op, const u32x4& v0c, f32x16& Oc) {
    u32x4 plo;
    pup[2] = cvtpk(Sn[12], Sn[13]); pup[3] = cvtpk(Sn[14], Sn[15]); PIN(pup); SBAR();
    APRIO_HI(); Sn = MFMA32(k1, q1, ZERO16); SBAR(); APRIO_LO();
    Sc[0] = EX(Sc[0]); Sc[1] = EX(Sc[1]); Sc[2] = EX(Sc[2]); Sc[3] = EX(Sc[3]); PIN(Sc); SBAR();
    APRIO_HI(); Op = MFMA32(v1p, pup, Op); SBAR(); APRIO_LO();
    Sc[4] = EX(Sc[4]); Sc[5] = EX(Sc[5]); Sc[6] = EX(Sc[6]); Sc[7] = EX(Sc[7]); plo[0] = cvtpk(Sc[0], Sc[1]); plo[1] = cvtpk(Sc[2], Sc[3]); PIN(Sc); PIN(plo); SBAR();
    APRIO_HI(); Sn = MFMA32(k2, q2, Sn); SBAR(); APRIO_LO();
    Sc[8] = EX(Sc[8]); Sc[9] = EX(Sc[9]); Sc[10] = EX(Sc[10]); Sc[11] = EX(Sc[11]); plo[2] = cvtpk(Sc[4], Sc[5]); plo[3] = cvtpk(Sc[6], Sc[7]); PIN(Sc); PIN(plo); SBAR();
    Sc[12] = EX(Sc[12]); Sc[13] = EX(Sc[13]); SBAR();
    APRIO_HI(); Oc = MFMA32(v0c, plo, Oc); SBAR(); APRIO_LO();
    Sc[14] = EX(Sc[14]); Sc[15] = EX(Sc[15]); pup[0] = cvtpk(Sc[8], Sc[9]); pup[1] = cvtpk(Sc[10], Sc[11]); PIN(Sc); PIN(pup); SBAR();
}
DI void att_path(lptr lds, const Params& P, const KvPre& kpre, int mt, int s, int a4, int lane, u64 tag, int bid) {
    char* DBGWS = P.ws; (void)DBGWS;
    unsigned cnt_v = (unsigned)(uintptr_t)(lds + L_CNT), one_v = 1u; PIN(cnt_v); PIN(one_v);
    kv_job(lds, kpre, a4, lane, mt, s, P, tag, cnt_v, one_v, bid);
    STAMP(26);
    {
        const int s0 = s & ~(XMAP - 1); constexpr int nl = XMAP * 324 * 9, per = (nl + 8191) / 8192; unsigned sink = 0u;
#pragma unroll
        for (int i = 0; i < per; ++i) { const int id = ((bid >> 3) * per + i) * 256 + a4 * 64 + lane;
            if (id < nl) { const int sl = id / 2916, rem = id - 2916 * sl;
                const char* ptr = rem < 2592 ? (const char*)P.wd + (size_t)(288 * (s0 + sl)) * 1152 + rem * 128 : (const char*)P.wo + (size_t)(36 * (s0 + sl)) * 1152 + (rem - 2592) * 128;
                sink ^= *(const unsigned*)ptr; } }
        asm volatile("" :: "v"(sink));
    }
    if (a4 == 0) { wait_flags<4, KV_SLEEP>(P.ws + WS_FB + (bid & 7) * 2048, 64, tag, lane);
        lds_add1(cnt_v + C_KVOK, one_v);
        if (bid == 0 && lane == 0) gst64(P.ws + WS_G, tag); }
    else lds_wait_ge(lds + L_CNT + C_KVOK, 1);
    STAMP(27);
    const int rh = a4 & 1, kh = a4 >> 1, r = lane & 31, h = lane >> 5;
    const __amdgpu_buffer_rsrc_t rs = mkrsrc(P.ws, WS_KV_END);
    const unsigned voK1 = WS_K1 + lane * 16, voK2 = h == 0 ? WS_K2 + r * 16 : OOB, voV = r < 19 ? WS_V + h * 304 + r * 16 : OOB;
#define LDK(B, kb) do { B.k1 = __builtin_amdgcn_raw_buffer_load_b128(rs, voK1, (kb) * 1024, LDAUX); B.k2 = __builtin_amdgcn_raw_buffer_load_b128(rs, voK2, (kb) * 512, LDAUX); } while (0)
#define LDV(B, kb) do { B.v0 = __builtin_amdgcn_raw_buffer_load_b128(rs, voV, (kb) * 1216, LDAUX); B.v1 = __builtin_amdgcn_raw_buffer_load_b128(rs, voV, (kb) * 1216 + 608, LDAUX); } while (0)
    KVf A, B;
    const int kb0 = 32 * kh;
    KVf R; LDK(R, 0);
    LDK(A, kb0); LDV(A, kb0); LDK(B, kb0 + 1);
    lds_wait_ge(lds + L_CNT + C_Q, 4);
    STAMP(1);
    u32x4 q1[4], q2[4];
#pragma unroll
    for (int T = 0; T < 4; ++T) { const int rt = 2 * rh + (T >> 1), a = T & 1;
        q1[T] = *(LAS u32x4*)(lds + L_Q + (rt * 2 + a) * 2048 + lane * 16); q2[T] = *(LAS u32x4*)(lds + L_Q + (rt * 2 + a) * 2048 + 1024 + lane * 16); }
    WAITL0();
#pragma unroll
    for (int T = 0; T < 4; ++T) { PIN(q1[T]); PIN(q2[T]); }
    lds_add1(cnt_v + C_QTAKEN, one_v);
    float mref[4];
#pragma unroll
    for (int T = 0; T < 4; ++T) {
        f32x16 s0 = MFMA32(R.k1, q1[T], ZERO16); s0 = MFMA32(R.k2, q2[T], s0);
        float m = s0[0];
#pragma unroll
        for (int i = 1; i < 16; ++i) m = fmaxf(m, s0[i]);
        m = swap32_max(m); const unsigned mb = cvtpk(-m, 0.f) & 0xffffu; mref[T] = -__uint_as_float(mb << 16);
        if (h == 0) q2[T][1] = mb;
    }
    STAMP(2);
    f32x16 O[4] = {}; f32x16 S0, S1 = {}; u32x4 pup = {0u, 0u, 0u, 0u};
    B.v1 = A.v1;
    S0 = MFMA32(A.k1, q1[0], ZERO16); S0 = MFMA32(A.k2, q2[0], S0);
    for (int it = 0; it < 16; ++it) {
        const int kb = kb0 + 2 * it;
        att_slot(S0, S1, pup, A.k1, A.k2, q1[1], q2[1], B.v1, O[3], A.v0, O[0]);
        LDV(B, kb + 1);
        att_slot(S1, S0, pup, A.k1, A.k2, q1[2], q2[2], A.v1, O[0], A.v0, O[1]);
        att_slot(S0, S1, pup, A.k1, A.k2, q1[3], q2[3], A.v1, O[1], A.v0, O[2]);
        { const int kn = (kb + 2 < 64) ? kb + 2 : 63; LDK(A, kn); }
        att_slot(S1, S0, pup, B.k1, B.k2, q1[0], q2[0], A.v1, O[2], A.v0, O[3]);
        att_slot(S0, S1, pup, B.k1, B.k2, q1[1], q2[1], A.v1, O[3], B.v0, O[0]);
        { const int kn = (kb + 2 < 64) ? kb + 2 : 63; LDV(A, kn); }
        att_slot(S1, S0, pup, B.k1, B.k2, q1[2], q2[2], B.v1, O[0], B.v0, O[1]);
        att_slot(S0, S1, pup, B.k1, B.k2, q1[3], q2[3], B.v1, O[1], B.v0, O[2]);
        { const int kn = (kb + 3 < 64) ? kb + 3 : 63; LDK(B, kn); }
        att_slot(S1, S0, pup, A.k1, A.k2, q1[0], q2[0], B.v1, O[2], B.v0, O[3]);
    }
    pup[2] = cvtpk(S1[12], S1[13]); pup[3] = cvtpk(S1[14], S1[15]);
    O[3] = MFMA32(B.v1, pup, O[3]);
    STAMP(9);
    BARRIER();
    {
        const __amdgpu_buffer_rsrc_t rsO = mkrsrc(P.wo, DM * DM * 2); const unsigned voD = (lane >> 3) * 1152 + (((lane & 7) ^ (((lane >> 4) & 1) << 2)) * 16), ring = (unsigned)(uintptr_t)(lds + L_RING);
        dma16(ring + (36 + a4) * 1024u, voD, rsO, (unsigned)(36 * s + 8 * a4) * 1152u);
        if (a4 < 2) dma16(ring + (40 + a4) * 1024u, voD, rsO, (unsigned)(36 * s + 8 * (a4 + 4)) * 1152u); }
    auto send = [&](const f32x16& o, float m, int rr) {
        lptr d = lds + L_XCH + (a4 * 2 + rr) * 3072 + lane * 16;
        *(LAS f32x4*)(d) = (f32x4){o[0], o[1], o[2], o[3]}; *(LAS f32x4*)(d + 1024) = (f32x4){o[4], o[5], o[6], o[7]}; *(LAS f32x4*)(d + 2048) = (f32x4){o[8], o[9], o[10], m}; };
    if (kh) { send(O[0], mref[0], 0); send(O[2], mref[2], 1); } else { send(O[1], mref[1], 0); send(O[3], mref[3], 1); }
    BARRIER();
    auto comb = [&](const f32x16& o, float m, int rr) {
        lptr d = lds + L_XCH + ((a4 ^ 2) * 2 + rr) * 3072 + lane * 16;
        const f32x4 a0 = *(LAS f32x4*)(d), a1 = *(LAS f32x4*)(d + 1024), a2 = *(LAS f32x4*)(d + 2048);
        const float M = fmaxf(m, a2[3]), fa = EX(m - M), fb = EX(a2[3] - M);
        float v[10];
#pragma unroll
        for (int i = 0; i < 4; ++i) { v[i] = o[i] * fa + a0[i] * fb; v[4 + i] = o[4 + i] * fa + a1[i] * fb; }
        v[8] = o[8] * fa + a2[0] * fb; v[9] = o[9] * fa + a2[1] * fb;
        float l = o[10] * fa + a2[2] * fb;
        l = __shfl(l, r, 64);
        const float il = __builtin_amdgcn_rcpf(l);
        lptr od = lds + L_O + (64 * rh + 32 * rr + r) * O_STRIDE + 36 * kh;
        *(LAS unsigned*)(od + 8 * h) = cvtpk(v[0] * il, v[1] * il); *(LAS unsigned*)(od + 8 * h + 4) = cvtpk(v[2] * il, v[3] * il);
        *(LAS unsigned*)(od + 16 + 8 * h) = cvtpk(v[4] * il, v[5] * il); *(LAS unsigned*)(od + 16 + 8 * h + 4) = cvtpk(v[6] * il, v[7] * il);
        if (h == 0) *(LAS unsigned*)(od + 32) = cvtpk(v[8] * il, v[9] * il);
        else if (kh == 1) { *(LAS unsigned*)(od + 36) = 0u; *(LAS unsigned*)(od + 40) = 0u; *(LAS unsigned*)(od + 44) = 0u; *(LAS unsigned*)(od + 48) = 0u; *(LAS unsigned*)(od + 52) = 0u; *(LAS unsigned*)(od + 56) = 0u; }
    };
    if (kh) { comb(O[1], mref[1], 0); comb(O[3], mref[3], 1); } else { comb(O[0], mref[0], 0); comb(O[2], mref[2], 1); }
}

struct PhB { __amdgpu_buffer_rsrc_t rsD, rsO, rsP; unsigned voD, vo5, ldsw, so0, so4, so5, po, d5, d5str; lptr tb0; u32x4 af[3];
             lptr tc, tn; unsigned vo_a, vo5_a, dst_a, d5_a, col_a, vo_b, vo5_b, dst_b, d5_b, col_b, pst, po_p; };
template <int NH, int I> DI void phb_piece(PhB& b, unsigned dst, unsigned d5, unsigned col, unsigned vo, unsigned vo5) {
    if constexpr (I < 4) dma16(dst + I * 8192, vo, b.rsD, b.so0 + I * (64u * 1152u) + col);
    else if constexpr (I == 4) { if constexpr (NH == 0) dma16(dst + I * 8192, vo, b.rsD, b.so4 + col); else dma16(dst + I * 8192, vo, b.rsO, b.so4 + col); }
    else if constexpr (NH == 0) dma16(d5, vo5, b.rsO, b.so5 + col);
}
template <int NH, int KT> DI void phb_issue(PhB& b) {
    constexpr int o = KT >= 19 ? KT - 19 : KT + 2;
    if constexpr (o < 18 && (o % 3) == (NH ? 2 : 0)) { if constexpr (KT >= 19) phb_piece<NH, o / 3>(b, b.dst_b, b.d5_b, b.col_b, b.vo_b, b.vo5_b); else phb_piece<NH, o / 3>(b, b.dst_a, b.d5_a, b.col_a, b.vo_a, b.vo5_a); }
}
template <int NH, int KT> DI void phb_step(PhB& b, const u32x4 (&hf)[21], f32x16& acc, f32x16& prev, u32x4& w0, u32x4& w1) {
    constexpr int P = NH ? 5 : 6;
    if constexpr (KT == 19) asm volatile("s_waitcnt vmcnt(%0) lgkmcnt(0)\n\ts_barrier" :: "i"(P + 2) : "memory");
    if constexpr (KT + 2 < 21) b.af[(KT + 2) % 3] = trfrag(b.tc + (KT + 2) * 2048, 1024); else b.af[(KT + 2) % 3] = trfrag(b.tn + (KT + 2 - 21) * 2048, 1024);
    SBAR();
    if constexpr (KT == 0) acc = MFMA32(b.af[0], hf[0], ZERO16); else acc = MFMA32(b.af[KT % 3], hf[KT], acc);
    phb_issue<NH, KT>(b);
    if constexpr (KT >= 2 && KT < 6) { constexpr int i = KT - 2;
        if constexpr (i < 2) { w0[2 * i] = cvtpk(prev[4 * i], prev[4 * i + 1]); w0[2 * i + 1] = cvtpk(prev[4 * i + 2], prev[4 * i + 3]); PIN(w0); }
        else { w1[2 * i - 4] = cvtpk(prev[4 * i], prev[4 * i + 1]); w1[2 * i - 3] = cvtpk(prev[4 * i + 2], prev[4 * i + 3]); PIN(w1); }
        if constexpr (i == 1) __builtin_amdgcn_raw_buffer_store_b128(w0, b.rsP, b.po_p, b.pst, SC1);
        if constexpr (i == 3) __builtin_amdgcn_raw_buffer_store_b128(w1, b.rsP, b.po_p + 1024, b.pst, SC1);
    }
    SBAR();
}
template <int NH, int... Ks> DI void phb_stage_(PhB& b, const u32x4 (&hf)[21], f32x16& acc, f32x16& prev, std::integer_sequence<int, Ks...>) {
    u32x4 w0, w1; (phb_step<NH, Ks>(b, hf, acc, prev, w0, w1), ...);
}
template <int NH> DI void phb_stage(PhB& b, const u32x4 (&hf)[21], f32x16& acc, f32x16& prev, int np) {
    const unsigned b0 = (unsigned)(np % 3) * BBUF, b1 = (unsigned)((np + 1) % 3) * BBUF, b2 = (unsigned)((np + 2) % 3) * BBUF;
    b.tc = b.tb0 + b0; b.tn = b.tb0 + b1;
    b.dst_a = b.ldsw + b2; b.d5_a = b.d5 + ((np + 2) % 3) * b.d5str; b.col_a = (np + 2) * 128u; b.vo_a = np + 2 < 9 ? b.voD : OOB; b.vo5_a = np + 2 < 9 ? b.vo5 : OOB;
    b.dst_b = b.ldsw + b0; b.d5_b = b.d5 + (np % 3) * b.d5str; b.col_b = (np + 3) * 128u; b.vo_b = np + 3 < 9 ? b.voD : OOB; b.vo5_b = np + 3 < 9 ? b.vo5 : OOB;
    b.pst = np > 0 ? (unsigned)(np - 1) * 4096u : 0u; b.po_p = np > 0 ? b.po : OOB;
    phb_stage_<NH>(b, hf, acc, prev, std::make_integer_sequence<int, 21>{});
}
template <int NH> DI void phb_run(PhB& b, const u32x4 (&hf)[21], char* DBGWS) {
    b.af[0] = trfrag(b.tb0, 1024); b.af[1] = trfrag(b.tb0 + 2048, 1024);
    phb_piece<NH, 0>(b, b.ldsw + BBUF, b.d5 + b.d5str, 128u, b.voD, b.vo5); phb_piece<NH, 1>(b, b.ldsw + BBUF, b.d5 + b.d5str, 128u, b.voD, b.vo5); phb_piece<NH, 2>(b, b.ldsw + BBUF, b.d5 + b.d5str, 128u, b.voD, b.vo5);
    phb_piece<NH, 3>(b, b.ldsw + BBUF, b.d5 + b.d5str, 128u, b.voD, b.vo5); phb_piece<NH, 4>(b, b.ldsw + BBUF, b.d5 + b.d5str, 128u, b.voD, b.vo5); phb_piece<NH, 5>(b, b.ldsw + BBUF, b.d5 + b.d5str, 128u, b.voD, b.vo5);
    if constexpr (NH == 0) phb_piece<NH, 0>(b, b.ldsw + 2 * BBUF, b.d5 + 2 * b.d5str, 256u, b.voD, b.vo5);
    f32x16 a0 = {}, a1 = {};
    for (int np = 0; np < 9; np += 2) {
        phb_stage<NH>(b, hf, a0, a1, np);
        STAMP(13 + np);
        if (np + 1 < 9) phb_stage<NH>(b, hf, a1, a0, np + 1);
        STAMP(14 + np);
    }
    u32x4 w0, w1;
#pragma unroll
    for (int i = 0; i < 4; ++i) { w0[i] = cvtpk(a0[2 * i], a0[2 * i + 1]); w1[i] = cvtpk(a0[8 + 2 * i], a0[9 + 2 * i]); }
    __builtin_amdgcn_raw_buffer_store_b128(w0, b.rsP, b.po, 8 * 4096, SC1); __builtin_amdgcn_raw_buffer_store_b128(w1, b.rsP, b.po + 1024, 8 * 4096, SC1);
}

__global__ __launch_bounds__(512, 2) void fused_kernel(Params P) {
    extern __shared__ __attribute__((aligned(16))) char smem[];
    lptr lds = (lptr)smem;
    const int tid = threadIdx.x, lane0 = tid & 63, wid = __builtin_amdgcn_readfirstlane(tid >> 6);
    const int bid = blockIdx.x;
    const int xg = bid & 7, jj = bid >> 3, s = 4 * (xg & 3) + (jj & 3), mt = 8 * (xg >> 2) + (jj >> 2);
    char* DBGWS = P.ws; (void)DBGWS;
    STAMPR(30); STAMP(0);
    STAMP(46);
    const u64 g0 = gld64(P.ws + WS_G);
    if (tid < 16) *(LAS unsigned*)(lds + L_CNT + 4 * tid) = 0u;
    {
        const __amdgpu_buffer_rsrc_t rsX = mkrsrc(P.x, SEQ * DM * 2);
        const unsigned ldsu0 = (unsigned)(uintptr_t)(lds + L_X) + wid * 18432u, so = (unsigned)(128 * mt + 16 * wid) * 1152u;
        unsigned row = 0u, cp = lane0;
#pragma unroll 2
        for (int i = 0; i < 18; ++i) {
            dma16(ldsu0 + i * 1024, row * 1152u + 16u * ((cp & ~7u) | ((cp & 7u) ^ ((row >> 1) & 7u))), rsX, so);
            cp += 64u; if (cp >= 72u) { cp -= 72u; row += 1u; } }
    }
    if (wid >= 4) {
        const int w = wid & 3; const __amdgpu_buffer_rsrc_t rsQ = mkrsrc(P.wq, DM * DM * 2);
        const unsigned vq = (lane0 >> 2) * 1152 + (lane0 & 3) * 16, ring = (unsigned)(uintptr_t)(lds + L_RING);
#pragma unroll
        for (int i = 0; i < 3; ++i) { const int pi = w + 4 * i; const bool up = pi >= 6; const int pr = up ? pi - 6 : pi;
            dma16(ring + (up ? 6144u : 0u) + pr * 1024u, vq, rsQ, 16u * pr * 1152u + (unsigned)s * 72u + (up ? 36u : 0u)); }
    }
    STAMP(47);
    KvPre kpre;
    if (wid < 4) kv_preload(kpre, tid, P);
    const u64 tag = g0 + 1ull;
    STAMP(32);
    if (wid < 4) WAITV(11); else WAITV(0);
    BARRIER();
    STAMP(34);
    if (wid >= 4) { ffn_path(lds, P, mt, s, wid & 3, lane0); BARRIER(); BARRIER(); }
    else att_path(lds, P, kpre, mt, s, wid & 3, lane0, tag, bid);
    int tz = threadIdx.x; asm volatile("" : "+v"(tz));
    const int lane = tz & 63;
    const int rt = wid & 3, nh = wid >> 2, r = lane & 31, h = lane >> 5, g4 = lane >> 4, q = (lane & 15) >> 2, p = lane & 3;
    PhB b;
    b.rsD = mkrsrc(P.wd, FF * DM * 2); b.rsO = mkrsrc(P.wo, DM * DM * 2);
    b.voD = (lane >> 3) * 1152 + (((lane & 7) ^ (((lane >> 4) & 1) << 2)) * 16); b.vo5 = wid < 2 ? b.voD : OOB;
    b.ldsw = (unsigned)(uintptr_t)(lds + L_RING) + wid * 1024;
    b.d5 = wid < 2 ? b.ldsw + 5 * 8192 : (unsigned)(uintptr_t)(lds + L_RING) + 3 * BBUF; b.d5str = wid < 2 ? BBUF : 0u;
    b.so0 = (unsigned)(288 * s + 8 * wid) * 1152u;
    b.so4 = wid < 4 ? (unsigned)(288 * s + 8 * (wid + 32)) * 1152u : (unsigned)(36 * s + 8 * (wid - 4)) * 1152u;
    b.so5 = (unsigned)(36 * s + 8 * (wid + 4)) * 1152u;
    const unsigned rowl = 4 * h + q; const unsigned ch = (4 * nh + 2 * (g4 & 1) + (p >> 1)) ^ (((rowl >> 1) & 1) << 2);
    b.tb0 = lds + L_RING + rowl * 128 + ch * 16 + 8 * (p & 1);
    b.rsP = mkrsrc(P.ws + WS_PART + (size_t)(mt * 16 + s) * SLAB, SLAB);
    b.po = (unsigned)(rt * 18 + nh) * 2048u + lane * 16;
    BARRIER();
    STAMP(23);
    u32x4 hf[21];
#pragma unroll
    for (int t = 0; t < 18; ++t) hf[t] = *(LAS u32x4*)(lds + L_H + ((t >> 1) * 4 + rt) * 2048 + (t & 1) * 1024 + lane * 16);
#pragma unroll
    for (int t = 0; t < 3; ++t) { lptr o = lds + L_O + (32 * rt + r) * O_STRIDE + 32 * t + 8 * h; const u32x2 a = *(LAS u32x2*)o, bb = *(LAS u32x2*)(o + 16); hf[18 + t] = (u32x4){a[0], a[1], bb[0], bb[1]}; }
    WAITL0();
#pragma unroll
    for (int t = 0; t < 21; ++t) PIN(hf[t]);
    WAITV(0);
    BARRIER();
    STAMP(24);
    if (nh) phb_run<1>(b, hf, DBGWS); else phb_run<0>(b, hf, DBGWS);
    STAMP(25);
    const int u0 = 9 * s + wid, tile0 = u0 >> 1, trt0 = tile0 / 18, tnt0 = tile0 - 18 * trt0;
    const size_t ro0 = (size_t)(128 * mt + 32 * trt0 + r) * DM + 32 * tnt0 + 16 * (u0 & 1) + 4 * h;
    const u32x2 x0 = *(const u32x2*)(P.x + ro0), x1 = *(const u32x2*)(P.x + ro0 + 8);
    const int u8 = 9 * s + 8, tile8 = u8 >> 1, trt8 = tile8 / 18, tnt8 = tile8 - 18 * trt8;
    const size_t ro8 = (size_t)(128 * mt + 32 * trt8 + r) * DM + 32 * tnt8 + 16 * (u8 & 1) + 4 * h;
    u32x2 y0 = {0u, 0u}, y1 = {0u, 0u};
    if (wid == 0) { y0 = *(const u32x2*)(P.x + ro8); y1 = *(const u32x2*)(P.x + ro8 + 8); }
    WAITV(0);
    STAMP(43);
    BARRIER();
    STAMP(44); STAMPR(45);
    if (wid == 0 && lane == 0) gst64(P.ws + WS_FC + mt * 128 + s * 8, tag);
    {   const __amdgpu_buffer_rsrc_t rp = mkrsrc(P.ws + WS_PART + (size_t)mt * 16 * SLAB, 16 * SLAB);
        u32x4 v[16], v8[16];
        lptr mask = lds + L_CNT + C_MASK;
        unsigned seen = 0u;
#pragma unroll
        for (int j = 0; j < 16; ++j) { const int k = (s + j) & 15;
            if (!((seen >> k) & 1u)) {
                for (unsigned n = 0; n < (1u << 18); ++n) {
                    if (wid == 1) { const bool ok = lane < 16 && gld64(P.ws + WS_FC + mt * 128 + lane * 8) == tag; seen = (unsigned)__ballot(ok);
                        if (lane == 0) __hip_atomic_store((LAS unsigned*)mask, seen, __ATOMIC_RELAXED, __HIP_MEMORY_SCOPE_WORKGROUP); }
                    else seen = (unsigned)__builtin_amdgcn_readfirstlane(lds_ld(mask));
                    if ((seen >> k) & 1u) break;
                    __builtin_amdgcn_s_sleep(2); }
                FENCE(); }
            v[j] = __builtin_amdgcn_raw_buffer_load_b128(rp, lane * 16, k * SLAB + u0 * 1024, SC1);
            if (wid == 0) v8[j] = __builtin_amdgcn_raw_buffer_load_b128(rp, lane * 16, k * SLAB + u8 * 1024, SC1);
        }
        STAMP(28);
        auto fin = [&](const u32x4 (&vv)[16], const u32x2& xa, const u32x2& xb, size_t ro) {
            float acc[8] = {0.f, 0.f, 0.f, 0.f, 0.f, 0.f, 0.f, 0.f};
#pragma unroll
            for (int k = 0; k < 16; ++k)
#pragma unroll
                for (int i = 0; i < 4; ++i) { acc[2 * i] += blo(vv[k][i]); acc[2 * i + 1] += bhi(vv[k][i]); }
            *(u32x2*)(P.out + ro) = (u32x2){cvtpk(acc[0] + blo(xa[0]), acc[1] + bhi(xa[0])), cvtpk(acc[2] + blo(xa[1]), acc[3] + bhi(xa[1]))};
            *(u32x2*)(P.out + ro + 8) = (u32x2){cvtpk(acc[4] + blo(xb[0]), acc[5] + bhi(xb[0])), cvtpk(acc[6] + blo(xb[1]), acc[7] + bhi(xb[1]))}; };
        fin(v, x0, x1, ro0);
        if (wid == 0) fin(v8, y0, y1, ro8);
    }
    STAMP(29); STAMPR(31);
}

extern "C" void kernel_launch(void* const* d_in, const int* in_sizes, int n_in, void* d_out, int out_size, void* d_ws, size_t ws_size, hipStream_t stream) {
    Params P; P.x = (const u16*)d_in[0]; P.wq = (const u16*)d_in[1]; P.wk = (const u16*)d_in[2]; P.wv = (const u16*)d_in[3]; P.wo = (const u16*)d_in[4];
    P.wg = (const u16*)d_in[6]; P.wu = (const u16*)d_in[7]; P.wd = (const u16*)d_in[8]; P.out = (u16*)d_out; P.ws = (char*)d_ws;
    (void)hipFuncSetAttribute((const void*)fused_kernel, hipFuncAttributeMaxDynamicSharedMemorySize, LDS_BYTES);
    fused_kernel<<<256, 512, LDS_BYTES, stream>>>(P);
}
```

```cpp
#include <hip/hip_runtime.h>
#include <stdint.h>
#include <utility>

#define XMAP 4
#define FFN_PRIO 1
#define ATT_TOGGLE 2
#define KV_PRIO 3
#define LDAUX 16
#define POLL_SLEEP 8
#define PF_AT 3
#define KV_SLEEP 8
#define DI __device__ __forceinline__
#define LAS __attribute__((address_space(3)))
#define GAS __attribute__((address_space(1)))
typedef unsigned short u16;
typedef unsigned long long u64;
typedef __bf16 bf16x8 __attribute__((ext_vector_type(8)));
typedef __bf16 bf16x2 __attribute__((ext_vector_type(2)));
typedef short s16x4 __attribute__((ext_vector_type(4)));
typedef float f32x2 __attribute__((ext_vector_type(2)));
typedef float f32x4 __attribute__((ext_vector_type(4)));
typedef float f32x16 __attribute__((ext_vector_type(16)));
typedef unsigned u32x2 __attribute__((ext_vector_type(2)));
typedef unsigned u32x4 __attribute__((ext_vector_type(4)));
typedef LAS char* lptr;

constexpr int SEQ = 2048, DM = 576, FF = 4608;
constexpr unsigned WS_K1 = 0, WS_K2 = 65536, WS_V = WS_K2 + 64 * 512, WS_KV_END = WS_V + 128 * 608;
constexpr unsigned WS_XN = 262144, XN_BYTES = SEQ * DM * 2;
constexpr unsigned WS_G = 3u << 20, WS_FA = WS_G + 4096, WS_FB = WS_G + 8192, WS_FC = WS_FB + 16384;
constexpr size_t WS_PART = 4u << 20;
constexpr unsigned SLAB = 4 * 18 * 2048;
constexpr unsigned SLOT = 12288, L_CNT = 0, L_RING = 4096, L_H = L_RING + 6 * SLOT, L_Q = L_H + 57344, L_O = L_H + 73728, O_STRIDE = 96, L_XCH = L_RING + 4 * SLOT, LDS_BYTES = 163840;
constexpr unsigned BBUF = 43008;
constexpr unsigned L_X = 16384, L_KVX = L_H + 43008, KV_XS = 1168, KV_LDS = 43008 + 8 * 1168;
static_assert(L_O + 128 * O_STRIDE <= LDS_BYTES && L_RING + 3 * BBUF + 1024 <= L_O && KV_LDS <= 57344 && L_X + 128 * 1152 <= LDS_BYTES , "LDS map");
constexpr unsigned C_RING = 0, C_Q = 4, C_QTAKEN = 8, C_KV1 = 12, C_KV2 = 16, C_KVOK = 20, C_ATTP = 32, C_MASK = 48;
constexpr unsigned OOB = 0x40000000u;
constexpr int SC1 = 16;
constexpr float QSCALE = 0.23570226039551584f * 1.4426950408889634f;

#define SBAR() __builtin_amdgcn_sched_barrier(0)
#define PIN(x) asm volatile("" : "+v"(x))
#define MFMA32(a, b, c) __builtin_amdgcn_mfma_f32_32x32x16_bf16(__builtin_bit_cast(bf16x8, a), __builtin_bit_cast(bf16x8, b), c, 0, 0, 0)
#define MFMA16(a, b, c) __builtin_amdgcn_mfma_f32_16x16x32_bf16(__builtin_bit_cast(bf16x8, a), __builtin_bit_cast(bf16x8, b), c, 0, 0, 0)
#define WAITV(n) asm volatile("s_waitcnt vmcnt(" #n ")" ::: "memory")
#define WAITL0() asm volatile("s_waitcnt lgkmcnt(0)" ::: "memory")
#define FENCE() asm volatile("" ::: "memory")
#define BARRIER() asm volatile("s_waitcnt lgkmcnt(0)\n\ts_barrier" ::: "memory")
#define ZERO16 ((f32x16){})
#define RLX_AGENT __ATOMIC_RELAXED, __HIP_MEMORY_SCOPE_AGENT

DI unsigned cvtpk(float lo, float hi) { f32x2 v = {lo, hi}; return __builtin_bit_cast(unsigned, __builtin_convertvector(v, bf16x2)); }
DI float bfr(float f) { return __uint_as_float(cvtpk(f, 0.f) << 16); }
DI float blo(unsigned v) { return __uint_as_float(v << 16); }
DI float bhi(unsigned v) { return __uint_as_float(v & 0xffff0000u); }
DI u32x2 trread(lptr p) { return __builtin_bit_cast(u32x2, __builtin_amdgcn_ds_read_tr16_b64_v4i16((LAS s16x4*)p)); }
DI u32x4 trfrag(lptr p, unsigned second) { u32x2 a = trread(p), b = trread(p + second); return (u32x4){a[0], a[1], b[0], b[1]}; }
DI __amdgpu_buffer_rsrc_t mkrsrc(const void* p, unsigned bytes) { return __builtin_amdgcn_make_buffer_rsrc((void*)p, (short)0, (int)bytes, 0x00020000); }
DI void dma16(unsigned ldsaddr, unsigned voff, __amdgpu_buffer_rsrc_t rs, unsigned soff) {
    unsigned keep;
    asm volatile("s_mov_b32 %0, m0\n\ts_mov_b32 m0, %1\n\ts_nop 0\n\tbuffer_load_dwordx4 %2, %3, %4 offen lds\n\ts_mov_b32 m0, %0"
                 : "=&s"(keep) : "s"(ldsaddr), "v"(voff), "s"(rs), "s"(soff) : "memory");
}
DI float swap32_max(float v) { auto r = __builtin_amdgcn_permlane32_swap(__float_as_uint(v), __float_as_uint(v), false, false); return fmaxf(__uint_as_float(r[0]), __uint_as_float(r[1])); }
DI void lds_add1(unsigned addr_v, unsigned one_v) { asm volatile("s_mov_b64 exec, 1\n\tds_add_u32 %0, %1\n\ts_mov_b64 exec, -1" :: "v"(addr_v), "v"(one_v) : "memory"); }
DI unsigned lds_ld(lptr p) { return __hip_atomic_load((LAS unsigned*)p, __ATOMIC_RELAXED, __HIP_MEMORY_SCOPE_WORKGROUP); }
DI void lds_wait_ge(lptr p, unsigned v) { for (unsigned n = 0; (unsigned)__builtin_amdgcn_readfirstlane(lds_ld(p)) < v && n < (1u << 22); ++n) __builtin_amdgcn_s_sleep(1); FENCE(); }
DI u64 gld64(const char* p) { return __hip_atomic_load((GAS u64*)p, RLX_AGENT); }
DI void gst64(char* p, u64 v) { __hip_atomic_store((GAS u64*)p, v, RLX_AGENT); }
template <int PER_LANE, int SLEEP = POLL_SLEEP> DI void wait_flags(const char* base, int nlanes, u64 tag, int lane) {
    for (unsigned n = 0; n < (1u << 18); ++n) { bool ok = true;
        if (lane < nlanes) {
#pragma unroll
            for (int i = 0; i < PER_LANE; ++i) ok &= gld64(base + (lane + 64 * i) * 8) == tag; }
        if (__all(ok)) break;
        __builtin_amdgcn_s_sleep(SLEEP); }
    FENCE();
}
DI void acquire_agent() { __builtin_amdgcn_fence(__ATOMIC_ACQUIRE, "agent"); WAITV(0); }

#define STAMP(i) do {} while (0)
#define STAMPR(i) do {} while (0)

struct Params { const u16* x; const u16* wq; const u16* wk; const u16* wv; const u16* wo; const u16* wg; const u16* wu; const u16* wd; u16* out; char* ws; };

template <int CTRL> DI float dpp_add(float v) { return v + __uint_as_float(__builtin_amdgcn_update_dpp(0u, __float_as_uint(v), CTRL, 0xf, 0xf, true)); }
DI float sum32(float v) {
    v = dpp_add<0xB1>(v); v = dpp_add<0x4E>(v); v = dpp_add<0x141>(v); v = dpp_add<0x140>(v);
    auto r = __builtin_amdgcn_permlane16_swap(__float_as_uint(v), __float_as_uint(v), false, false); return __uint_as_float(r[0]) + __uint_as_float(r[1]);
}
constexpr unsigned KV_WV = 21 * 1024;
struct KvPre { u32x4 w[11]; };
DI void kv_preload(KvPre& k, int t256, const Params& P) {
    const __amdgpu_buffer_rsrc_t rk = mkrsrc(P.wk, DM * 18 * 2), rv = mkrsrc(P.wv, DM * 18 * 2);
#pragma unroll
    for (int i = 0; i < 11; ++i) { const int id = t256 + 256 * i;
        if (i < 5) k.w[i] = __builtin_amdgcn_raw_buffer_load_b128(rk, id * 16, 0, 0);
        else if (i == 5) k.w[i] = id < 1296 ? __builtin_amdgcn_raw_buffer_load_b128(rk, id * 16, 0, 0) : __builtin_amdgcn_raw_buffer_load_b128(rv, (id - 1296) * 16, 0, 0);
        else k.w[i] = __builtin_amdgcn_raw_buffer_load_b128(rv, (id - 1296) * 16, 0, 0); }
}
DI void kv_job(lptr lds, const KvPre& k, int a, int lane, int mt, int s, const Params& P, u64 tag, unsigned cnt_v, unsigned one_v, int bid) {
    char* ws = P.ws; char* DBGWS = ws; (void)DBGWS;
    lptr cnt = lds + L_CNT;
    __builtin_amdgcn_s_setprio(KV_PRIO);
    const int hr = lane >> 5, c = lane & 31, rr = 2 * a + hr, lrow = 8 * s + rr;
    unsigned xv[9];
#pragma unroll
    for (int j = 0; j < 9; ++j) { const unsigned dw = c + 32 * j, ch = dw >> 2; xv[j] = *(LAS unsigned*)(lds + L_X + lrow * 1152 + (((ch & ~7u) | ((ch & 7u) ^ ((lrow >> 1) & 7u))) << 4) + 4 * (dw & 3)); }
    float sum = 0.f;
#pragma unroll
    for (int j = 0; j < 9; ++j) sum += blo(xv[j]) + bhi(xv[j]);
    const float mean = sum32(sum) * (1.f / DM);
    float sq = 0.f;
#pragma unroll
    for (int j = 0; j < 9; ++j) { const float d0 = blo(xv[j]) - mean, d1 = bhi(xv[j]) - mean; sq = __builtin_fmaf(d0, d0, sq); sq = __builtin_fmaf(d1, d1, sq); }
    const float mb = bfr(mean), sd = bfr(__builtin_amdgcn_sqrtf(bfr(bfr(sum32(sq) * (1.f / DM)) + 1e-5f))), rinv = __builtin_amdgcn_rcpf(sd);
#pragma unroll
    for (int j = 0; j < 9; ++j) xv[j] = cvtpk(bfr(blo(xv[j]) - mb) * rinv, bfr(bhi(xv[j]) - mb) * rinv);
    STAMP(33);
    BARRIER();
#pragma unroll
    for (int j = 0; j < 9; ++j) *(LAS unsigned*)(lds + L_KVX + rr * KV_XS + 4 * (c + 32 * j)) = xv[j];
    { const int t256 = a * 64 + lane;
#pragma unroll
      for (int i = 0; i < 11; ++i) { const int id = t256 + 256 * i; if (id < 2592) *(LAS u32x4*)(lds + L_H + (id < 1296 ? id * 16 : KV_WV + (id - 1296) * 16)) = k.w[i]; } }
    WAITL0();
    lds_add1(cnt_v + C_KV1, one_v);
    lds_wait_ge(cnt + C_KV1, 4);
    STAMP(36);
    const int i16 = lane & 15, g = lane >> 4;
    const int col = (a & 1) ? (16 + (i16 < 2 ? i16 : 1)) : i16;
    lptr wp = lds + L_H + (a < 2 ? 0u : KV_WV) + (8 * g) * 36 + 2 * col;
    lptr xp = lds + L_KVX + (i16 & 7) * KV_XS + 16 * g;
    f32x4 acc = {0.f, 0.f, 0.f, 0.f};
    {
        u32x4 wf[18], xq[18];
#pragma unroll
        for (int t = 0; t < 18; ++t) {
#pragma unroll
            for (int j = 0; j < 4; ++j) wf[t][j] = (unsigned)*(LAS u16*)(wp + t * 32 * 36 + (2 * j) * 36) | ((unsigned)*(LAS u16*)(wp + t * 32 * 36 + (2 * j + 1) * 36) << 16);
            xq[t] = *(LAS u32x4*)(xp + t * 64); }
#pragma unroll
        for (int t = 0; t < 18; ++t) { PIN(wf[t]); PIN(xq[t]); }
        if (a < 2) {
#pragma unroll
            for (int t = 0; t < 18; ++t) acc = MFMA16(wf[t], xq[t], acc);
        } else {
#pragma unroll
            for (int t = 0; t < 18; ++t) acc = MFMA16(xq[t], wf[t], acc);
        }
    }
    const __amdgpu_buffer_rsrc_t rs = mkrsrc(ws, WS_KV_END);
    const int ky0 = 128 * mt + 8 * s;
    const unsigned p01 = cvtpk(acc[0], acc[1]), p23 = cvtpk(acc[2], acc[3]);
    if (a == 0) { if (i16 < 8) { const int ky = ky0 + i16; __builtin_amdgcn_raw_buffer_store_b64((u32x2){p01, p23}, rs, WS_K1 + (ky >> 5) * 1024 + ((ky & 31) + 32 * (g & 1)) * 16 + 8 * (g >> 1), 0, SC1); } }
    else if (a == 1) { if (i16 < 8 && g == 0) { const int ky = ky0 + i16; __builtin_amdgcn_raw_buffer_store_b128((u32x4){p01, 0x00003F80u, 0u, 0u}, rs, WS_K2 + (ky >> 5) * 512 + (ky & 31) * 16, 0, SC1); } }
    else { const int cc = (a == 2 ? 0 : 16) + i16; const bool ones = (cc == 18);
        if (g < 2 && cc <= 18) __builtin_amdgcn_raw_buffer_store_b64(ones ? (u32x2){0x3F803F80u, 0x3F803F80u} : (u32x2){p01, p23}, rs, WS_V + (ky0 >> 4) * 608 + g * 304 + cc * 16 + 8 * (s & 1), 0, SC1); }
    __builtin_amdgcn_s_setprio(0);
    STAMP(37);
    WAITV(0);
    STAMP(38);
    lds_add1(cnt_v + C_KV2, one_v);
    if (a == 0) { lds_wait_ge(cnt + C_KV2, 4);
        if (lane < 8) gst64(ws + WS_FB + lane * 2048 + bid * 8, tag); }
}

struct Tgt { __amdgpu_buffer_rsrc_t rs[3]; unsigned base[3], ld[3], jstr; unsigned vo; };
struct Ffn {
    u32x4 xf[36];
    u32x4 ag[2], au[2];
    lptr rb0, rb1;
    Tgt cur, nxt;
    lptr lds; unsigned sync_target; unsigned cnt_v, one_v;
};
template <int Q> DI void ffn_dma(const Tgt& t, int j) { dma16(t.ld[Q] + j * SLOT, t.vo, t.rs[Q], t.base[Q] + j * t.jstr); }
template <int T, int U> DI void ffn_read1(Ffn& f) {
    constexpr int j = T / 6, kk = T % 6; lptr b = (j < 3 ? f.rb0 : f.rb1) + (j % 3) * SLOT + kk * 1024 + U * 6144;
    if constexpr (U) f.au[T % 2] = trfrag(b, 256); else f.ag[T % 2] = trfrag(b, 256);
}
template <int T> DI void ffn_issue(Ffn& f) {
    constexpr int j = T / 6, kk = T % 6;
    if constexpr (kk == 4 || kk == 0 || kk == 2) {
        constexpr int q = kk == 4 ? 0 : kk == 0 ? 1 : 2; constexpr int m = (kk == 4 ? j : j - 1) + 5;
        if constexpr (m < 6) ffn_dma<q>(f.cur, m); else ffn_dma<q>(f.nxt, m - 6);
    }
}
#define SYNC_ON true
template <int T> DI void ffn_norm(Ffn& f, f32x2 r2, f32x2 c2) {
    float lo[4], hi[4];
#pragma unroll
    for (int i = 0; i < 4; ++i) { lo[i] = blo(f.xf[T][i]); hi[i] = bhi(f.xf[T][i]); }
    SBAR();
    f32x2 t[4];
#pragma unroll
    for (int i = 0; i < 4; ++i) t[i] = __builtin_elementwise_fma((f32x2){lo[i], hi[i]}, r2, c2);
    SBAR();
#pragma unroll
    for (int i = 0; i < 4; ++i) f.xf[T][i] = cvtpk(t[i][0], t[i][1]);
    PIN(f.xf[T]);
}
template <bool FIRST, int T> DI void ffn_step(Ffn& f, f32x16& g, f32x16& u, unsigned& polled, f32x2 r2, f32x2 c2, const f32x16& pg, const f32x16& pu, u32x4& w0, u32x4& w1, lptr hp) {
    constexpr int kk = T % 6;
    if constexpr (SYNC_ON && kk == 1) {
        WAITV(8);
        lds_add1(f.cnt_v + C_RING, f.one_v);
        f.sync_target += 4;
    }
    if constexpr (SYNC_ON && kk == 3) polled = lds_ld(f.lds + L_CNT + C_RING);
    if constexpr (SYNC_ON && kk == 4) {
        if (__builtin_expect((unsigned)__builtin_amdgcn_readfirstlane(polled) < f.sync_target, 0)) {
            for (unsigned n = 0; (unsigned)__builtin_amdgcn_readfirstlane(lds_ld(f.lds + L_CNT + C_RING)) < f.sync_target && n < (1u << 22); ++n) __builtin_amdgcn_s_sleep(1); }
        FENCE();
    }
    ffn_read1<(T + 1) % 36, 0>(f);
    SBAR();
    if constexpr (T == 0) g = MFMA32(f.ag[0], f.xf[0], ZERO16); else g = MFMA32(f.ag[T % 2], f.xf[T], g);
    ffn_issue<T>(f);
    SBAR();
    ffn_read1<(T + 1) % 36, 1>(f);
    SBAR();
    if constexpr (T == 0) u = MFMA32(f.au[0], f.xf[0], ZERO16); else u = MFMA32(f.au[T % 2], f.xf[T], u);
    if constexpr (FIRST && T + 2 < 36) ffn_norm<T + 2>(f, r2, c2);
    if constexpr (!FIRST && T >= 2 && T < 10) { constexpr int i = T - 2;
        const unsigned v = cvtpk(__builtin_fmaxf(pg[2 * i], 0.f) * pu[2 * i], __builtin_fmaxf(pg[2 * i + 1], 0.f) * pu[2 * i + 1]);
        if constexpr (i < 4) { w0[i] = v; PIN(w0); } else { w1[i - 4] = v; PIN(w1); }
        if constexpr (i == 3) *(LAS u32x4*)hp = w0;
        if constexpr (i == 7) *(LAS u32x4*)(hp + 1024) = w1; }
    SBAR();
}
template <bool FIRST, int... Ts> DI void ffn_chunk_(Ffn& f, f32x16& g, f32x16& u, f32x2 r2, f32x2 c2, const f32x16& pg, const f32x16& pu, lptr hp, std::integer_sequence<int, Ts...>) { unsigned polled = 0; u32x4 w0, w1; (ffn_step<FIRST, Ts>(f, g, u, polled, r2, c2, pg, pu, w0, w1, hp), ...); }

DI void ffn_path(lptr lds, const Params& P, int mt, int s, int w, int lane) {
    char* DBGWS = P.ws; (void)DBGWS;
    Ffn f; f.lds = lds;
    const int h = lane >> 5, g4 = lane >> 4, q = (lane & 15) >> 2, p = lane & 3;
    const unsigned ring = (unsigned)(uintptr_t)(lds + L_RING);
    const __amdgpu_buffer_rsrc_t rsG = mkrsrc(P.wg, DM * FF * 2), rsU = mkrsrc(P.wu, DM * FF * 2), rs1 = mkrsrc(w < 2 ? P.wg : P.wu, DM * FF * 2);
    { const __amdgpu_buffer_rsrc_t rsQ = mkrsrc(P.wq, DM * DM * 2);
#pragma unroll
      for (int i = 0; i < 3; ++i) { const int pi = w + 4 * i; const bool up = pi >= 6; const int pr = up ? pi - 6 : pi; const unsigned row = 16u * pr;
          f.cur.ld[i] = ring + (up ? 6144u : 0u) + pr * 1024u; f.cur.rs[i] = rsQ; f.cur.base[i] = row * 1152u + (unsigned)s * 72u + (up ? 36u : 0u);
          f.nxt.ld[i] = f.cur.ld[i]; f.nxt.base[i] = row * 9216u + (unsigned)s * 576u; }
      f.cur.jstr = 96u * 1152u; f.cur.vo = (lane >> 2) * 1152 + (lane & 3) * 16;
      f.nxt.rs[0] = rsG; f.nxt.rs[1] = rs1; f.nxt.rs[2] = rsU; f.nxt.jstr = 96u * 9216u; f.nxt.vo = (lane >> 2) * 9216 + (lane & 3) * 16; }
    f.rb0 = lds + L_RING + (8 * h + q) * 64 + (g4 & 1) * 32 + 8 * p; { lptr t1 = f.rb0 + 3 * SLOT; PIN(t1); f.rb1 = t1; }
    { unsigned cv = (unsigned)(uintptr_t)(lds + L_CNT), ov = 1u; PIN(cv); PIN(ov); f.cnt_v = cv; f.one_v = ov; }
    f32x16 sA = {}, sB = {}; f32x2 r2, c2;
    {
        const int r = lane & 31; const unsigned gx = (r >> 1) & 7; lptr xb = lds + L_X + (32 * w + r) * 1152;
        lptr xb4[4];
#pragma unroll
        for (int i = 0; i < 4; ++i) xb4[i] = xb + 16 * ((2 * i + h) ^ gx);
#pragma unroll
        for (int t = 0; t < 36; ++t) f.xf[t] = *(LAS u32x4*)(xb4[t & 3] + 128 * (t >> 2));
    }
    const u32x4 ones = {0x3F803F80u, 0x3F803F80u, 0x3F803F80u, 0x3F803F80u};
#pragma unroll
    for (int t = 0; t < 16; ++t) { sA = MFMA32(ones, f.xf[t], sA); sB = MFMA32(f.xf[t], f.xf[t], sB); PIN(sA); PIN(sB); SBAR(); }
    BARRIER();
    STAMP(26);
#define STAT(t) do { sA = MFMA32(ones, f.xf[t], sA); sB = MFMA32(f.xf[t], f.xf[t], sB); PIN(sA); PIN(sB); SBAR(); } while (0)
    STAT(16); STAT(17); STAT(18);
    ffn_dma<0>(f.cur, 1); STAT(19); ffn_dma<1>(f.cur, 1); STAT(20); ffn_dma<2>(f.cur, 1); STAT(21);
    ffn_dma<0>(f.cur, 2); STAT(22); ffn_dma<1>(f.cur, 2); STAT(23); ffn_dma<2>(f.cur, 2); STAT(24);
    ffn_dma<0>(f.cur, 3); STAT(25); ffn_dma<1>(f.cur, 3); STAT(26); ffn_dma<2>(f.cur, 3); STAT(27);
    ffn_dma<0>(f.cur, 4); STAT(28); STAT(29); STAT(30); STAT(31); STAT(32); STAT(33); STAT(34); STAT(35);
#undef STAT
    {
        const float rowsum = sA[0];
        const int r = lane & 31, reg = (r & 3) + 4 * (r >> 3);
        float dg = sB[0];
#pragma unroll
        for (int i = 1; i < 16; ++i) dg = reg == i ? sB[i] : dg;
        dg = ((r >> 2) & 1) == h ? dg : 0.f;
        auto sw = __builtin_amdgcn_permlane32_swap(__float_as_uint(dg), __float_as_uint(dg), false, false);
        const float sq = __uint_as_float(sw[0]) + __uint_as_float(sw[1]);
        const float mean = rowsum * (1.f / DM); float var = fmaxf(sq * (1.f / DM) - mean * mean, 0.f);
        if (__builtin_expect(__any(mean * mean > 256.f * var), 0)) {
            float v2 = 0.f;
#pragma unroll
            for (int t = 0; t < 36; ++t) { PIN(f.xf[t]); PIN(v2);
#pragma unroll
                for (int i = 0; i < 4; ++i) { const float d0 = blo(f.xf[t][i]) - mean, d1 = bhi(f.xf[t][i]) - mean; v2 = __builtin_fmaf(d0, d0, v2); v2 = __builtin_fmaf(d1, d1, v2); } }
            auto s2 = __builtin_amdgcn_permlane32_swap(__float_as_uint(v2), __float_as_uint(v2), false, false);
            var = (__uint_as_float(s2[0]) + __uint_as_float(s2[1])) * (1.f / DM);
        }
        const float mb = bfr(mean), sd = bfr(__builtin_amdgcn_sqrtf(bfr(bfr(var) + 1e-5f)));
        const float rinv = __builtin_amdgcn_rcpf(sd), cc = -mb * rinv; r2 = (f32x2){rinv, rinv}; c2 = (f32x2){cc, cc};
    }
    PIN(r2); PIN(c2);
#pragma unroll
    for (int t = 0; t < 36; ++t) PIN(f.xf[t]);
    ffn_norm<0>(f, r2, c2); ffn_norm<1>(f, r2, c2);
    STAMP(27);
    f.sync_target = 0;
    STAMP(2);
    ffn_read1<0, 0>(f); ffn_read1<0, 1>(f);
    __builtin_amdgcn_s_setprio(FFN_PRIO);
    auto next_params = [&](int c) {
        f.cur = f.nxt;
        if (c < 8) {
#pragma unroll
            for (int i = 0; i < 3; ++i) f.nxt.base[i] += 64u;
        } else if (c == 8) {
            const __amdgpu_buffer_rsrc_t rsD = mkrsrc(P.wd, FF * DM * 2);
#pragma unroll
            for (int i = 0; i < 3; ++i) { f.nxt.rs[i] = rsD; f.nxt.ld[i] = ring + 1024u * (w + 4 * i); f.nxt.base[i] = (288u * s + 8u * (w + 4 * i)) * 1152u; }
            f.nxt.jstr = 96u * 1152u; f.nxt.vo = (lane >> 3) * 1152 + (((lane & 7) ^ (((lane >> 4) & 1) << 2)) * 16);
        }
    };
    {
        f32x16 g, u;
        ffn_chunk_<true>(f, g, u, r2, c2, g, u, lds, std::make_integer_sequence<int, 36>{});
        const bool lo = lane < 32;
#pragma unroll
        for (int a = 0; a < 2; ++a) { const f32x16& qq = a ? u : g; u32x4 f0, f1;
#pragma unroll
            for (int i = 0; i < 4; ++i) f0[i] = cvtpk(qq[2 * i] * QSCALE, qq[2 * i + 1] * QSCALE);
            f1 = (u32x4){lo ? cvtpk(qq[8] * QSCALE, qq[9] * QSCALE) : 0u, 0u, 0u, 0u};
            *(LAS u32x4*)(lds + L_Q + (w * 2 + a) * 2048 + lane * 16) = f0;
            *(LAS u32x4*)(lds + L_Q + (w * 2 + a) * 2048 + 1024 + lane * 16) = f1; }
        WAITL0();
        lds_add1(f.cnt_v + C_Q, f.one_v);
        next_params(0);
        STAMP(3);
    }
    lptr hw = lds + L_H + w * 2048 + lane * 16;
    f32x16 gA, uA, gB = {}, uB = {};
    for (int c = 1; c < 10; c += 2) {
        if (c == 9) lds_wait_ge(lds + L_CNT + C_QTAKEN, 4);
        ffn_chunk_<false>(f, gA, uA, r2, c2, gB, uB, c == 1 ? lds + L_O + w * 2048 + lane * 16 : hw + (c - 2) * 8192, std::make_integer_sequence<int, 36>{});
        next_params(c);
        STAMP(3 + c);
        if (c + 1 < 10) {
            if (c == 1) lds_wait_ge(lds + L_CNT + C_KV2, 4);
            ffn_chunk_<false>(f, gB, uB, r2, c2, gA, uA, hw + (c - 1) * 8192, std::make_integer_sequence<int, 36>{});
            next_params(c + 1);
            STAMP(4 + c);
        }
    }
    {
        u32x4 w0, w1;
#pragma unroll
        for (int i = 0; i < 4; ++i) { w0[i] = cvtpk(__builtin_fmaxf(gA[2 * i], 0.f) * uA[2 * i], __builtin_fmaxf(gA[2 * i + 1], 0.f) * uA[2 * i + 1]);
                                      w1[i] = cvtpk(__builtin_fmaxf(gA[8 + 2 * i], 0.f) * uA[8 + 2 * i], __builtin_fmaxf(gA[9 + 2 * i], 0.f) * uA[9 + 2 * i]); }
        *(LAS u32x4*)(hw + 8 * 8192) = w0; *(LAS u32x4*)(hw + 8 * 8192 + 1024) = w1;
    }
    __builtin_amdgcn_s_setprio(0);
    WAITV(0);
}

struct KVf { u32x4 k1, k2, v0, v1; };
#define EX(x) __builtin_amdgcn_exp2f(x)
#define APRIO_HI() __builtin_amdgcn_s_setprio(ATT_TOGGLE)
#define APRIO_LO() __builtin_amdgcn_s_setprio(0)
DI void att_slot(f32x16& Sc, f32x16& Sn, u32x4& pup, const u32x4& k1, const u32x4& k2, const u32x4& q1, const u32x4& q2,
                 const u32x4& v1p, f32x16& Op, const u32x4& v0c, f32x16& Oc) {
    u32x4 plo;
    pup[2] = cvtpk(Sn[12], Sn[13]); pup[3] = cvtpk(Sn[14], Sn[15]); PIN(pup); SBAR();
    APRIO_HI(); Sn = MFMA32(k1, q1, ZERO16); SBAR(); APRIO_LO();
    Sc[0] = EX(Sc[0]); Sc[1] = EX(Sc[1]); Sc[2] = EX(Sc[2]); Sc[3] = EX(Sc[3]); PIN(Sc); SBAR();
    APRIO_HI(); Op = MFMA32(v1p, pup, Op); SBAR(); APRIO_LO();
    Sc[4] = EX(Sc[4]); Sc[5] = EX(Sc[5]); Sc[6] = EX(Sc[6]); Sc[7] = EX(Sc[7]); plo[0] = cvtpk(Sc[0], Sc[1]); plo[1] = cvtpk(Sc[2], Sc[3]); PIN(Sc); PIN(plo); SBAR();
    APRIO_HI(); Sn = MFMA32(k2, q2, Sn); SBAR(); APRIO_LO();
    Sc[8] = EX(Sc[8]); Sc[9] = EX(Sc[9]); Sc[10] = EX(Sc[10]); Sc[11] = EX(Sc[11]); plo[2] = cvtpk(Sc[4], Sc[5]); plo[3] = cvtpk(Sc[6], Sc[7]); PIN(Sc); PIN(plo); SBAR();
    Sc[12] = EX(Sc[12]); Sc[13] = EX(Sc[13]); SBAR();
    APRIO_HI(); Oc = MFMA32(v0c, plo, Oc); SBAR(); APRIO_LO();
    Sc[14] = EX(Sc[14]); Sc[15] = EX(Sc[15]); pup[0] = cvtpk(Sc[8], Sc[9]); pup[1] = cvtpk(Sc[10], Sc[11]); PIN(Sc); PIN(pup); SBAR();
}
DI void att_path(lptr lds, const Params& P, const KvPre& kpre, int mt, int s, int a4, int lane, u64 tag, int bid) {
    char* DBGWS = P.ws; (void)DBGWS;
    unsigned cnt_v = (unsigned)(uintptr_t)(lds + L_CNT), one_v = 1u; PIN(cnt_v); PIN(one_v);
    kv_job(lds, kpre, a4, lane, mt, s, P, tag, cnt_v, one_v, bid);
    STAMP(26);
    {
        const int s0 = s & ~(XMAP - 1); constexpr int nl = XMAP * 324 * 9, per = (nl + 8191) / 8192; unsigned sink = 0u;
#pragma unroll
        for (int i = 0; i < per; ++i) { const int id = ((bid >> 3) * per + i) * 256 + a4 * 64 + lane;
            if (id < nl) { const int sl = id / 2916, rem = id - 2916 * sl;
                const char* ptr = rem < 2592 ? (const char*)P.wd + (size_t)(288 * (s0 + sl)) * 1152 + rem * 128 : (const char*)P.wo + (size_t)(36 * (s0 + sl)) * 1152 + (rem - 2592) * 128;
                sink ^= *(const unsigned*)ptr; } }
        asm volatile("" :: "v"(sink));
    }
    if (a4 == 0) { wait_flags<4, KV_SLEEP>(P.ws + WS_FB + (bid & 7) * 2048, 64, tag, lane);
        lds_add1(cnt_v + C_KVOK, one_v);
        if (bid == 0 && lane == 0) gst64(P.ws + WS_G, tag); }
    else lds_wait_ge(lds + L_CNT + C_KVOK, 1);
    STAMP(27);
    const int rh = a4 & 1, kh = a4 >> 1, r = lane & 31, h = lane >> 5;
    const __amdgpu_buffer_rsrc_t rs = mkrsrc(P.ws, WS_KV_END);
    const unsigned voK1 = WS_K1 + lane * 16, voK2 = h == 0 ? WS_K2 + r * 16 : OOB, voV = r < 19 ? WS_V + h * 304 + r * 16 : OOB;
#define LDK(B, kb) do { B.k1 = __builtin_amdgcn_raw_buffer_load_b128(rs, voK1, (kb) * 1024, LDAUX); B.k2 = __builtin_amdgcn_raw_buffer_load_b128(rs, voK2, (kb) * 512, LDAUX); } while (0)
#define LDV(B, kb) do { B.v0 = __builtin_amdgcn_raw_buffer_load_b128(rs, voV, (kb) * 1216, LDAUX); B.v1 = __builtin_amdgcn_raw_buffer_load_b128(rs, voV, (kb) * 1216 + 608, LDAUX); } while (0)
    KVf A, B;
    const int kb0 = 32 * kh;
    KVf R; LDK(R, 0);
    LDK(A, kb0); LDV(A, kb0); LDK(B, kb0 + 1);
    lds_wait_ge(lds + L_CNT + C_Q, 4);
    STAMP(1);
    u32x4 q1[4], q2[4];
#pragma unroll
    for (int T = 0; T < 4; ++T) { const int rt = 2 * rh + (T >> 1), a = T & 1;
        q1[T] = *(LAS u32x4*)(lds + L_Q + (rt * 2 + a) * 2048 + lane * 16); q2[T] = *(LAS u32x4*)(lds + L_Q + (rt * 2 + a) * 2048 + 1024 + lane * 16); }
    WAITL0();
#pragma unroll
    for (int T = 0; T < 4; ++T) { PIN(q1[T]); PIN(q2[T]); }
    lds_add1(cnt_v + C_QTAKEN, one_v);
    float mref[4];
#pragma unroll
    for (int T = 0; T < 4; ++T) {
        f32x16 s0 = MFMA32(R.k1, q1[T], ZERO16); s0 = MFMA32(R.k2, q2[T], s0);
        float m = s0[0];
#pragma unroll
        for (int i = 1; i < 16; ++i) m = fmaxf(m, s0[i]);
        m = swap32_max(m); const unsigned mb = cvtpk(-m, 0.f) & 0xffffu; mref[T] = -__uint_as_float(mb << 16);
        if (h == 0) q2[T][1] = mb;
    }
    STAMP(2);
    f32x16 O[4] = {}; f32x16 S0, S1 = {}; u32x4 pup = {0u, 0u, 0u, 0u};
    B.v1 = A.v1;
    S0 = MFMA32(A.k1, q1[0], ZERO16); S0 = MFMA32(A.k2, q2[0], S0);
    for (int it = 0; it < 16; ++it) {
        const int kb = kb0 + 2 * it;
        att_slot(S0, S1, pup, A.k1, A.k2, q1[1], q2[1], B.v1, O[3], A.v0, O[0]);
        LDV(B, kb + 1);
        att_slot(S1, S0, pup, A.k1, A.k2, q1[2], q2[2], A.v1, O[0], A.v0, O[1]);
        att_slot(S0, S1, pup, A.k1, A.k2, q1[3], q2[3], A.v1, O[1], A.v0, O[2]);
        { const int kn = (kb + 2 < 64) ? kb + 2 : 63; LDK(A, kn); }
        att_slot(S1, S0, pup, B.k1, B.k2, q1[0], q2[0], A.v1, O[2], A.v0, O[3]);
        att_slot(S0, S1, pup, B.k1, B.k2, q1[1], q2[1], A.v1, O[3], B.v0, O[0]);
        { const int kn = (kb + 2 < 64) ? kb + 2 : 63; LDV(A, kn); }
        att_slot(S1, S0, pup, B.k1, B.k2, q1[2], q2[2], B.v1, O[0], B.v0, O[1]);
        att_slot(S0, S1, pup, B.k1, B.k2, q1[3], q2[3], B.v1, O[1], B.v0, O[2]);
        { const int kn = (kb + 3 < 64) ? kb + 3 : 63; LDK(B, kn); }
        att_slot(S1, S0, pup, A.k1, A.k2, q1[0], q2[0], B.v1, O[2], B.v0, O[3]);
    }
    pup[2] = cvtpk(S1[12], S1[13]); pup[3] = cvtpk(S1[14], S1[15]);
    O[3] = MFMA32(B.v1, pup, O[3]);
    {
        bool bad = false;
#pragma unroll
        for (int T = 0; T < 4; ++T) { float mx = 0.f;
#pragma unroll
            for (int i = 0; i < 10; ++i) mx = fmaxf(mx, __builtin_fabsf(O[T][i]));
            bad |= !(O[T][10] < 0x1p60f) || !(mx < 0x1p120f) ;
#pragma unroll
            for (int i = 0; i < 10; ++i) bad |= O[T][i] != O[T][i]; }
        if (__builtin_expect(__any(bad), 0)) {
#pragma unroll
            for (int T = 0; T < 4; ++T) { O[T] = ZERO16; mref[T] = -__builtin_inff(); if (h == 0) q2[T][1] = 0u; }
            for (int kb = kb0; kb < kb0 + 32; ++kb) {
                KVf C; LDK(C, kb); LDV(C, kb);
#pragma unroll
                for (int T = 0; T < 4; ++T) {
                    f32x16 sc = MFMA32(C.k1, q1[T], ZERO16); sc = MFMA32(C.k2, q2[T], sc);
                    float m = sc[0];
#pragma unroll
                    for (int i = 1; i < 16; ++i) m = fmaxf(m, sc[i]);
                    m = fmaxf(swap32_max(m), mref[T]);
                    const float al = EX(mref[T] - m); mref[T] = m;
#pragma unroll
                    for (int i = 0; i < 16; ++i) { O[T][i] *= al; sc[i] = EX(sc[i] - m); }
                    const u32x4 p0 = {cvtpk(sc[0], sc[1]), cvtpk(sc[2], sc[3]), cvtpk(sc[4], sc[5]), cvtpk(sc[6], sc[7])}, p1 = {cvtpk(sc[8], sc[9]), cvtpk(sc[10], sc[11]), cvtpk(sc[12], sc[13]), cvtpk(sc[14], sc[15])};
                    O[T] = MFMA32(C.v0, p0, O[T]); O[T] = MFMA32(C.v1, p1, O[T]);
                }
            }
        }
    }
    STAMP(9);
    BARRIER();
    {
        const __amdgpu_buffer_rsrc_t rsO = mkrsrc(P.wo, DM * DM * 2); const unsigned voD = (lane >> 3) * 1152 + (((lane & 7) ^ (((lane >> 4) & 1) << 2)) * 16), ring = (unsigned)(uintptr_t)(lds + L_RING);
        dma16(ring + (36 + a4) * 1024u, voD, rsO, (unsigned)(36 * s + 8 * a4) * 1152u);
        if (a4 < 2) dma16(ring + (40 + a4) * 1024u, voD, rsO, (unsigned)(36 * s + 8 * (a4 + 4)) * 1152u); }
    auto send = [&](const f32x16& o, float m, int rr) {
        lptr d = lds + L_XCH + (a4 * 2 + rr) * 3072 + lane * 16;
        *(LAS f32x4*)(d) = (f32x4){o[0], o[1], o[2], o[3]}; *(LAS f32x4*)(d + 1024) = (f32x4){o[4], o[5], o[6], o[7]}; *(LAS f32x4*)(d + 2048) = (f32x4){o[8], o[9], o[10], m}; };
    if (kh) { send(O[0], mref[0], 0); send(O[2], mref[2], 1); } else { send(O[1], mref[1], 0); send(O[3], mref[3], 1); }
    BARRIER();
    auto comb = [&](const f32x16& o, float m, int rr) {
        lptr d = lds + L_XCH + ((a4 ^ 2) * 2 + rr) * 3072 + lane * 16;
        const f32x4 a0 = *(LAS f32x4*)(d), a1 = *(LAS f32x4*)(d + 1024), a2 = *(LAS f32x4*)(d + 2048);
        const float M = fmaxf(m, a2[3]), fa = EX(m - M), fb = EX(a2[3] - M);
        float v[10];
#pragma unroll
        for (int i = 0; i < 4; ++i) { v[i] = o[i] * fa + a0[i] * fb; v[4 + i] = o[4 + i] * fa + a1[i] * fb; }
        v[8] = o[8] * fa + a2[0] * fb; v[9] = o[9] * fa + a2[1] * fb;
        float l = o[10] * fa + a2[2] * fb;
        l = __shfl(l, r, 64);
        const float il = __builtin_amdgcn_rcpf(l);
        lptr od = lds + L_O + (64 * rh + 32 * rr + r) * O_STRIDE + 36 * kh;
        *(LAS unsigned*)(od + 8 * h) = cvtpk(v[0] * il, v[1] * il); *(LAS unsigned*)(od + 8 * h + 4) = cvtpk(v[2] * il, v[3] * il);
        *(LAS unsigned*)(od + 16 + 8 * h) = cvtpk(v[4] * il, v[5] * il); *(LAS unsigned*)(od + 16 + 8 * h + 4) = cvtpk(v[6] * il, v[7] * il);
        if (h == 0) *(LAS unsigned*)(od + 32) = cvtpk(v[8] * il, v[9] * il);
        else if (kh == 1) { *(LAS unsigned*)(od + 36) = 0u; *(LAS unsigned*)(od + 40) = 0u; *(LAS unsigned*)(od + 44) = 0u; *(LAS unsigned*)(od + 48) = 0u; *(LAS unsigned*)(od + 52) = 0u; *(LAS unsigned*)(od + 56) = 0u; }
    };
    if (kh) { comb(O[1], mref[1], 0); comb(O[3], mref[3], 1); } else { comb(O[0], mref[0], 0); comb(O[2], mref[2], 1); }
}

struct PhB { __amdgpu_buffer_rsrc_t rsD, rsO, rsP; unsigned voD, vo5, ldsw, so0, so4, so5, po, d5, d5str; lptr tb0; u32x4 af[7];
             lptr tc, tn; unsigned vo_a, vo5_a, dst_a, d5_a, col_a, vo_b, vo5_b, dst_b, d5_b, col_b, pst, po_p; };
template <int NH, int I> DI void phb_piece(PhB& b, unsigned dst, unsigned d5, unsigned col, unsigned vo, unsigned vo5) {
    if constexpr (I < 4) dma16(dst + I * 8192, vo, b.rsD, b.so0 + I * (64u * 1152u) + col);
    else if constexpr (I == 4) { if constexpr (NH == 0) dma16(dst + I * 8192, vo, b.rsD, b.so4 + col); else dma16(dst + I * 8192, vo, b.rsO, b.so4 + col); }
    else if constexpr (NH == 0) dma16(d5, vo5, b.rsO, b.so5 + col);
}
template <int NH, int KT> DI void phb_issue(PhB& b) {
    constexpr int o = KT >= 17 ? KT - 17 : KT + 4;
    if constexpr (o < 18 && (o % 3) == (NH ? 2 : 0)) { if constexpr (KT >= 17) phb_piece<NH, o / 3>(b, b.dst_b, b.d5_b, b.col_b, b.vo_b, b.vo5_b); else phb_piece<NH, o / 3>(b, b.dst_a, b.d5_a, b.col_a, b.vo_a, b.vo5_a); }
}
template <int NH, int KT> DI void phb_step(PhB& b, const u32x4 (&hf)[21], f32x16& acc, f32x16& prev, u32x4& w0, u32x4& w1) {
    constexpr int P = NH ? 5 : 6;
    if constexpr (KT == 17) asm volatile("s_waitcnt vmcnt(%0) lgkmcnt(0)\n\ts_barrier" :: "i"(P + 2) : "memory");
    if constexpr (KT + 4 < 21) b.af[(KT + 4) % 7] = trfrag(b.tc + (KT + 4) * 2048, 1024); else b.af[(KT + 4) % 7] = trfrag(b.tn + (KT + 4 - 21) * 2048, 1024);
    SBAR();
    if constexpr (KT == 0) acc = MFMA32(b.af[0], hf[0], ZERO16); else acc = MFMA32(b.af[KT % 7], hf[KT], acc);
    phb_issue<NH, KT>(b);
    if constexpr (KT >= 2 && KT < 6) { constexpr int i = KT - 2;
        if constexpr (i < 2) { w0[2 * i] = cvtpk(prev[4 * i], prev[4 * i + 1]); w0[2 * i + 1] = cvtpk(prev[4 * i + 2], prev[4 * i + 3]); PIN(w0); }
        else { w1[2 * i - 4] = cvtpk(prev[4 * i], prev[4 * i + 1]); w1[2 * i - 3] = cvtpk(prev[4 * i + 2], prev[4 * i + 3]); PIN(w1); }
        if constexpr (i == 1) __builtin_amdgcn_raw_buffer_store_b128(w0, b.rsP, b.po_p, b.pst, SC1);
        if constexpr (i == 3) __builtin_amdgcn_raw_buffer_store_b128(w1, b.rsP, b.po_p + 1024, b.pst, SC1);
    }
    SBAR();
}
template <int NH, int... Ks> DI void phb_stage_(PhB& b, const u32x4 (&hf)[21], f32x16& acc, f32x16& prev, std::integer_sequence<int, Ks...>) {
    u32x4 w0, w1; (phb_step<NH, Ks>(b, hf, acc, prev, w0, w1), ...);
}
template <int NH> DI void phb_stage(PhB& b, const u32x4 (&hf)[21], f32x16& acc, f32x16& prev, int np) {
    const unsigned b0 = (unsigned)(np % 3) * BBUF, b1 = (unsigned)((np + 1) % 3) * BBUF, b2 = (unsigned)((np + 2) % 3) * BBUF;
    b.tc = b.tb0 + b0; b.tn = b.tb0 + b1;
    b.dst_a = b.ldsw + b2; b.d5_a = b.d5 + ((np + 2) % 3) * b.d5str; b.col_a = (np + 2) * 128u; b.vo_a = np + 2 < 9 ? b.voD : OOB; b.vo5_a = np + 2 < 9 ? b.vo5 : OOB;
    b.dst_b = b.ldsw + b0; b.d5_b = b.d5 + (np % 3) * b.d5str; b.col_b = (np + 3) * 128u; b.vo_b = np + 3 < 9 ? b.voD : OOB; b.vo5_b = np + 3 < 9 ? b.vo5 : OOB;
    b.pst = np > 0 ? (unsigned)(np - 1) * 4096u : 0u; b.po_p = np > 0 ? b.po : OOB;
    phb_stage_<NH>(b, hf, acc, prev, std::make_integer_sequence<int, 21>{});
}
template <int NH> DI void phb_run(PhB& b, const u32x4 (&hf)[21], char* DBGWS) {
    b.af[0] = trfrag(b.tb0, 1024); b.af[1] = trfrag(b.tb0 + 2048, 1024); b.af[2] = trfrag(b.tb0 + 4096, 1024); b.af[3] = trfrag(b.tb0 + 6144, 1024);
    phb_piece<NH, 0>(b, b.ldsw + BBUF, b.d5 + b.d5str, 128u, b.voD, b.vo5); phb_piece<NH, 1>(b, b.ldsw + BBUF, b.d5 + b.d5str, 128u, b.voD, b.vo5); phb_piece<NH, 2>(b, b.ldsw + BBUF, b.d5 + b.d5str, 128u, b.voD, b.vo5);
    phb_piece<NH, 3>(b, b.ldsw + BBUF, b.d5 + b.d5str, 128u, b.voD, b.vo5); phb_piece<NH, 4>(b, b.ldsw + BBUF, b.d5 + b.d5str, 128u, b.voD, b.vo5); phb_piece<NH, 5>(b, b.ldsw + BBUF, b.d5 + b.d5str, 128u, b.voD, b.vo5);
    phb_piece<NH, 0>(b, b.ldsw + 2 * BBUF, b.d5 + 2 * b.d5str, 256u, b.voD, b.vo5);
    if constexpr (NH == 0) phb_piece<NH, 1>(b, b.ldsw + 2 * BBUF, b.d5 + 2 * b.d5str, 256u, b.voD, b.vo5);
    f32x16 a0 = {}, a1 = {};
    for (int np = 0; np < 9; np += 2) {
        phb_stage<NH>(b, hf, a0, a1, np);
        STAMP(13 + np);
        if (np + 1 < 9) phb_stage<NH>(b, hf, a1, a0, np + 1);
        STAMP(14 + np);
    }
    u32x4 w0, w1;
#pragma unroll
    for (int i = 0; i < 4; ++i) { w0[i] = cvtpk(a0[2 * i], a0[2 * i + 1]); w1[i] = cvtpk(a0[8 + 2 * i], a0[9 + 2 * i]); }
    __builtin_amdgcn_raw_buffer_store_b128(w0, b.rsP, b.po, 8 * 4096, SC1); __builtin_amdgcn_raw_buffer_store_b128(w1, b.rsP, b.po + 1024, 8 * 4096, SC1);
}

__global__ __launch_bounds__(512, 2) void fused_kernel(Params P) {
    extern __shared__ __attribute__((aligned(16))) char smem[];
    lptr lds = (lptr)smem;
    const int tid = threadIdx.x, lane0 = tid & 63, wid = __builtin_amdgcn_readfirstlane(tid >> 6);
    const int bid = blockIdx.x;
    const int xg = bid & 7, jj = bid >> 3, s = 4 * (xg & 3) + (jj & 3), mt = 8 * (xg >> 2) + (jj >> 2);
    char* DBGWS = P.ws; (void)DBGWS;
    STAMPR(30); STAMP(0);
    STAMP(46);
    const u64 g0 = gld64(P.ws + WS_G);
    if (tid < 16) *(LAS unsigned*)(lds + L_CNT + 4 * tid) = 0u;
    {
        const __amdgpu_buffer_rsrc_t rsX = mkrsrc(P.x, SEQ * DM * 2);
        const unsigned ldsu0 = (unsigned)(uintptr_t)(lds + L_X) + wid * 18432u, so = (unsigned)(128 * mt + 16 * wid) * 1152u;
        unsigned row = 0u, cp = lane0;
#pragma unroll 2
        for (int i = 0; i < 18; ++i) {
            dma16(ldsu0 + i * 1024, row * 1152u + 16u * ((cp & ~7u) | ((cp & 7u) ^ ((row >> 1) & 7u))), rsX, so);
            cp += 64u; if (cp >= 72u) { cp -= 72u; row += 1u; } }
    }
    if (wid >= 4) {
        const int w = wid & 3; const __amdgpu_buffer_rsrc_t rsQ = mkrsrc(P.wq, DM * DM * 2);
        const unsigned vq = (lane0 >> 2) * 1152 + (lane0 & 3) * 16, ring = (unsigned)(uintptr_t)(lds + L_RING);
#pragma unroll
        for (int i = 0; i < 3; ++i) { const int pi = w + 4 * i; const bool up = pi >= 6; const int pr = up ? pi - 6 : pi;
            dma16(ring + (up ? 6144u : 0u) + pr * 1024u, vq, rsQ, 16u * pr * 1152u + (unsigned)s * 72u + (up ? 36u : 0u)); }
    }
    STAMP(47);
    KvPre kpre;
    if (wid < 4) kv_preload(kpre, tid, P);
    const u64 tag = g0 + 1ull;
    STAMP(32);
    if (wid < 4) WAITV(11); else WAITV(0);
    BARRIER();
    STAMP(34);
    u32x4 hf[21];
    if (wid >= 4) { ffn_path(lds, P, mt, s, wid & 3, lane0); BARRIER();
#pragma unroll
        for (int t = 0; t < 18; ++t) hf[t] = *(LAS u32x4*)(lds + L_H + ((t >> 1) * 4 + (wid & 3)) * 2048 + (t & 1) * 1024 + lane0 * 16);
        BARRIER(); }
    else att_path(lds, P, kpre, mt, s, wid & 3, lane0, tag, bid);
    int tz = threadIdx.x; asm volatile("" : "+v"(tz));
    const int lane = tz & 63;
    const int rt = wid & 3, nh = wid >> 2, r = lane & 31, h = lane >> 5, g4 = lane >> 4, q = (lane & 15) >> 2, p = lane & 3;
    PhB b;
    b.rsD = mkrsrc(P.wd, FF * DM * 2); b.rsO = mkrsrc(P.wo, DM * DM * 2);
    b.voD = (lane >> 3) * 1152 + (((lane & 7) ^ (((lane >> 4) & 1) << 2)) * 16); b.vo5 = wid < 2 ? b.voD : OOB;
    b.ldsw = (unsigned)(uintptr_t)(lds + L_RING) + wid * 1024;
    b.d5 = wid < 2 ? b.ldsw + 5 * 8192 : (unsigned)(uintptr_t)(lds + L_RING) + 3 * BBUF; b.d5str = wid < 2 ? BBUF : 0u;
    b.so0 = (unsigned)(288 * s + 8 * wid) * 1152u;
    b.so4 = wid < 4 ? (unsigned)(288 * s + 8 * (wid + 32)) * 1152u : (unsigned)(36 * s + 8 * (wid - 4)) * 1152u;
    b.so5 = (unsigned)(36 * s + 8 * (wid + 4)) * 1152u;
    const unsigned rowl = 4 * h + q; const unsigned ch = (4 * nh + 2 * (g4 & 1) + (p >> 1)) ^ (((rowl >> 1) & 1) << 2);
    b.tb0 = lds + L_RING + rowl * 128 + ch * 16 + 8 * (p & 1);
    b.rsP = mkrsrc(P.ws + WS_PART + (size_t)(mt * 16 + s) * SLAB, SLAB);
    b.po = (unsigned)(rt * 18 + nh) * 2048u + lane * 16;
    BARRIER();
    STAMP(23);
    if (wid < 4) {
#pragma unroll
        for (int t = 0; t < 18; ++t) hf[t] = *(LAS u32x4*)(lds + L_H + ((t >> 1) * 4 + rt) * 2048 + (t & 1) * 1024 + lane * 16); }
#pragma unroll
    for (int t = 0; t < 3; ++t) { lptr o = lds + L_O + (32 * rt + r) * O_STRIDE + 32 * t + 8 * h; const u32x2 a = *(LAS u32x2*)o, bb = *(LAS u32x2*)(o + 16); hf[18 + t] = (u32x4){a[0], a[1], bb[0], bb[1]}; }
    WAITL0();
#pragma unroll
    for (int t = 0; t < 21; ++t) PIN(hf[t]);
    WAITV(0);
    BARRIER();
    STAMP(24);
    if (nh) phb_run<1>(b, hf, DBGWS); else phb_run<0>(b, hf, DBGWS);
    STAMP(25);
    const int u0 = 9 * s + wid, tile0 = u0 >> 1, trt0 = tile0 / 18, tnt0 = tile0 - 18 * trt0;
    const size_t ro0 = (size_t)(128 * mt + 32 * trt0 + r) * DM + 32 * tnt0 + 16 * (u0 & 1) + 4 * h;
    const u32x2 x0 = *(const u32x2*)(P.x + ro0), x1 = *(const u32x2*)(P.x + ro0 + 8);
    const int u8 = 9 * s + 8, tile8 = u8 >> 1, trt8 = tile8 / 18, tnt8 = tile8 - 18 * trt8;
    const size_t ro8 = (size_t)(128 * mt + 32 * trt8 + r) * DM + 32 * tnt8 + 16 * (u8 & 1) + 4 * h;
    u32x2 y0 = {0u, 0u}, y1 = {0u, 0u};
    if (wid == 0) { y0 = *(const u32x2*)(P.x + ro8); y1 = *(const u32x2*)(P.x + ro8 + 8); }
    WAITV(0);
    STAMP(43);
    BARRIER();
    STAMP(44); STAMPR(45);
    if (wid == 0 && lane == 0) gst64(P.ws + WS_FC + mt * 128 + s * 8, tag);
    {   const __amdgpu_buffer_rsrc_t rp = mkrsrc(P.ws + WS_PART + (size_t)mt * 16 * SLAB, 16 * SLAB);
        u32x4 v[16], v8[16];
        lptr mask = lds + L_CNT + C_MASK;
        unsigned seen = 0u;
#pragma unroll
        for (int j = 0; j < 16; ++j) { const int k = (s + j) & 15;
            if (!((seen >> k) & 1u)) {
                for (unsigned n = 0; n < (1u << 18); ++n) {
                    if (wid == 1) { const bool ok = lane < 16 && gld64(P.ws + WS_FC + mt * 128 + lane * 8) == tag; seen = (unsigned)__ballot(ok);
                        if (lane == 0) __hip_atomic_store((LAS unsigned*)mask, seen, __ATOMIC_RELAXED, __HIP_MEMORY_SCOPE_WORKGROUP); }
                    else seen = (unsigned)__builtin_amdgcn_readfirstlane(lds_ld(mask));
                    if ((seen >> k) & 1u) break;
                    __builtin_amdgcn_s_sleep(2); }
                FENCE(); }
            v[j] = __builtin_amdgcn_raw_buffer_load_b128(rp, lane * 16, k * SLAB + u0 * 1024, SC1);
            if (wid == 0) v8[j] = __builtin_amdgcn_raw_buffer_load_b128(rp, lane * 16, k * SLAB + u8 * 1024, SC1);
        }
        STAMP(28);
        auto fin = [&](const u32x4 (&vv)[16], const u32x2& xa, const u32x2& xb, size_t ro) {
            float acc[8] = {0.f, 0.f, 0.f, 0.f, 0.f, 0.f, 0.f, 0.f};
#pragma unroll
            for (int k = 0; k < 16; ++k)
#pragma unroll
                for (int i = 0; i < 4; ++i) { acc[2 * i] += blo(vv[k][i]); acc[2 * i + 1] += bhi(vv[k][i]); }
            *(u32x2*)(P.out + ro) = (u32x2){cvtpk(acc[0] + blo(xa[0]), acc[1] + bhi(xa[0])), cvtpk(acc[2] + blo(xa[1]), acc[3] + bhi(xa[1]))};
            *(u32x2*)(P.out + ro + 8) = (u32x2){cvtpk(acc[4] + blo(xb[0]), acc[5] + bhi(xb[0])), cvtpk(acc[6] + blo(xb[1]), acc[7] + bhi(xb[1]))}; };
        fin(v, x0, x1, ro0);
        if (wid == 0) fin(v8, y0, y1, ro8);
    }
    STAMP(29); STAMPR(31);
}

extern "C" void kernel_launch(void* const* d_in, const int* in_sizes, int n_in, void* d_out, int out_size, void* d_ws, size_t ws_size, hipStream_t stream) {
    Params P; P.x = (const u16*)d_in[0]; P.wq = (const u16*)d_in[1]; P.wk = (const u16*)d_in[2]; P.wv = (const u16*)d_in[3]; P.wo = (const u16*)d_in[4];
    P.wg = (const u16*)d_in[6]; P.wu = (const u16*)d_in[7]; P.wd = (const u16*)d_in[8]; P.out = (u16*)d_out; P.ws = (char*)d_ws;
    (void)hipFuncSetAttribute((const void*)fused_kernel, hipFuncAttributeMaxDynamicSharedMemorySize, LDS_BYTES);
    fused_kernel<<<256, 512, LDS_BYTES, stream>>>(P);
}
```

```cpp
#include <hip/hip_runtime.h>
#include <stdint.h>
#include <utility>

#define XMAP 4
#define FFN_PRIO 1
#define ATT_TOGGLE 2
#define KV_PRIO 3
#define LDAUX 16
#define POLL_SLEEP 8
#define PF_AT 3
#define KV_SLEEP 8
#define DI __device__ __forceinline__
#define LAS __attribute__((address_space(3)))
#define GAS __attribute__((address_space(1)))
typedef unsigned short u16;
typedef unsigned long long u64;
typedef __bf16 bf16x8 __attribute__((ext_vector_type(8)));
typedef __bf16 bf16x2 __attribute__((ext_vector_type(2)));
typedef short s16x4 __attribute__((ext_vector_type(4)));
typedef float f32x2 __attribute__((ext_vector_type(2)));
typedef float f32x4 __attribute__((ext_vector_type(4)));
typedef float f32x16 __attribute__((ext_vector_type(16)));
typedef unsigned u32x2 __attribute__((ext_vector_type(2)));
typedef unsigned u32x4 __attribute__((ext_vector_type(4)));
typedef LAS char* lptr;

constexpr int SEQ = 2048, DM = 576, FF = 4608;
constexpr unsigned WS_K1 = 0, WS_K2 = 65536, WS_V = WS_K2 + 64 * 512, WS_KV_END = WS_V + 128 * 608;
constexpr unsigned WS_XN = 262144, XN_BYTES = SEQ * DM * 2;
constexpr unsigned WS_G = 3u << 20, WS_FA = WS_G + 4096, WS_FB = WS_G + 8192, WS_FC = WS_FB + 16384, WS_FE = WS_FC + 4096;
constexpr size_t WS_PART = 4u << 20;
constexpr unsigned SLAB = 4 * 18 * 2048;
constexpr unsigned SLOT = 12288, L_CNT = 0, L_RING = 4096, L_H = L_RING + 6 * SLOT, L_Q = L_H + 57344, L_O = L_H + 73728, O_STRIDE = 96, L_XCH = L_RING + 4 * SLOT, LDS_BYTES = 163840;
constexpr unsigned BBUF = 43008;
constexpr unsigned L_X = 16384, L_KVX = L_H + 43008, KV_XS = 1168, KV_LDS = 43008 + 8 * 1168;
static_assert(L_O + 128 * O_STRIDE <= LDS_BYTES && L_RING + 3 * BBUF + 1024 <= L_O && KV_LDS <= 57344 && L_X + 128 * 1152 <= LDS_BYTES , "LDS map");
constexpr unsigned C_RING = 0, C_Q = 4, C_QTAKEN = 8, C_KV1 = 12, C_KV2 = 16, C_KVOK = 20, C_ATTP = 32, C_MASK = 48;
constexpr unsigned OOB = 0x40000000u;
constexpr int SC1 = 16;
constexpr float QSCALE = 0.23570226039551584f * 1.4426950408889634f;

#define SBAR() __builtin_amdgcn_sched_barrier(0)
#define PIN(x) asm volatile("" : "+v"(x))
#define MFMA32(a, b, c) __builtin_amdgcn_mfma_f32_32x32x16_bf16(__builtin_bit_cast(bf16x8, a), __builtin_bit_cast(bf16x8, b), c, 0, 0, 0)
#define MFMA16(a, b, c) __builtin_amdgcn_mfma_f32_16x16x32_bf16(__builtin_bit_cast(bf16x8, a), __builtin_bit_cast(bf16x8, b), c, 0, 0, 0)
#define WAITV(n) asm volatile("s_waitcnt vmcnt(" #n ")" ::: "memory")
#define WAITL0() asm volatile("s_waitcnt lgkmcnt(0)" ::: "memory")
#define FENCE() asm volatile("" ::: "memory")
#define BARRIER() asm volatile("s_waitcnt lgkmcnt(0)\n\ts_barrier" ::: "memory")
#define ZERO16 ((f32x16){})
#define RLX_AGENT __ATOMIC_RELAXED, __HIP_MEMORY_SCOPE_AGENT

DI unsigned cvtpk(float lo, float hi) { f32x2 v = {lo, hi}; return __builtin_bit_cast(unsigned, __builtin_convertvector(v, bf16x2)); }
DI float bfr(float f) { return __uint_as_float(cvtpk(f, 0.f) << 16); }
DI float blo(unsigned v) { return __uint_as_float(v << 16); }
DI float bhi(unsigned v) { return __uint_as_float(v & 0xffff0000u); }
DI u32x2 trread(lptr p) { return __builtin_bit_cast(u32x2, __builtin_amdgcn_ds_read_tr16_b64_v4i16((LAS s16x4*)p)); }
DI u32x4 trfrag(lptr p, unsigned second) { u32x2 a = trread(p), b = trread(p + second); return (u32x4){a[0], a[1], b[0], b[1]}; }
DI __amdgpu_buffer_rsrc_t mkrsrc(const void* p, unsigned bytes) { return __builtin_amdgcn_make_buffer_rsrc((void*)p, (short)0, (int)bytes, 0x00020000); }
DI void dma16(unsigned ldsaddr, unsigned voff, __amdgpu_buffer_rsrc_t rs, unsigned soff) {
    asm volatile("s_mov_b32 m0, %0\n\ts_nop 0\n\tbuffer_load_dwordx4 %1, %2, %3 offen lds" :: "s"(ldsaddr), "v"(voff), "s"(rs), "s"(soff) : "memory");
}
DI float swap32_max(float v) { auto r = __builtin_amdgcn_permlane32_swap(__float_as_uint(v), __float_as_uint(v), false, false); return fmaxf(__uint_as_float(r[0]), __uint_as_float(r[1])); }
DI void lds_add1(unsigned addr_v, unsigned one_v) { asm volatile("s_mov_b64 exec, 1\n\tds_add_u32 %0, %1\n\ts_mov_b64 exec, -1" :: "v"(addr_v), "v"(one_v) : "memory"); }
DI unsigned lds_ld(lptr p) { return __hip_atomic_load((LAS unsigned*)p, __ATOMIC_RELAXED, __HIP_MEMORY_SCOPE_WORKGROUP); }
DI void lds_wait_ge(lptr p, unsigned v) { for (unsigned n = 0; (unsigned)__builtin_amdgcn_readfirstlane(lds_ld(p)) < v && n < (1u << 22); ++n) __builtin_amdgcn_s_sleep(1); FENCE(); }
DI u64 gld64(const char* p) { return __hip_atomic_load((GAS u64*)p, RLX_AGENT); }
DI void gst64(char* p, u64 v) { __hip_atomic_store((GAS u64*)p, v, RLX_AGENT); }
template <int PER_LANE, int SLEEP = POLL_SLEEP> DI void wait_flags(const char* base, int nlanes, u64 tag, int lane) {
    for (unsigned n = 0; n < (1u << 18); ++n) { bool ok = true;
        if (lane < nlanes) {
#pragma unroll
            for (int i = 0; i < PER_LANE; ++i) ok &= gld64(base + (lane + 64 * i) * 8) == tag; }
        if (__all(ok)) break;
        __builtin_amdgcn_s_sleep(SLEEP); }
    FENCE();
}
DI void acquire_agent() { __builtin_amdgcn_fence(__ATOMIC_ACQUIRE, "agent"); WAITV(0); }

#define STAMP(i) do {} while (0)
#define STAMPR(i) do {} while (0)

struct Params { const u16* x; const u16* wq; const u16* wk; const u16* wv; const u16* wo; const u16* wg; const u16* wu; const u16* wd; u16* out; char* ws; };

template <int CTRL> DI float dpp_add(float v) { return v + __uint_as_float(__builtin_amdgcn_update_dpp(0u, __float_as_uint(v), CTRL, 0xf, 0xf, true)); }
DI float sum32(float v) {
    v = dpp_add<0xB1>(v); v = dpp_add<0x4E>(v); v = dpp_add<0x141>(v); v = dpp_add<0x140>(v);
    auto r = __builtin_amdgcn_permlane16_swap(__float_as_uint(v), __float_as_uint(v), false, false); return __uint_as_float(r[0]) + __uint_as_float(r[1]);
}
constexpr unsigned KV_WV = 21 * 1024;
struct KvPre { u32x4 w[11]; };
DI void kv_preload(KvPre& k, int t256, const Params& P) {
    const __amdgpu_buffer_rsrc_t rk = mkrsrc(P.wk, DM * 18 * 2), rv = mkrsrc(P.wv, DM * 18 * 2);
#pragma unroll
    for (int i = 0; i < 11; ++i) { const int id = t256 + 256 * i;
        if (i < 5) k.w[i] = __builtin_amdgcn_raw_buffer_load_b128(rk, id * 16, 0, 0);
        else if (i == 5) k.w[i] = id < 1296 ? __builtin_amdgcn_raw_buffer_load_b128(rk, id * 16, 0, 0) : __builtin_amdgcn_raw_buffer_load_b128(rv, (id - 1296) * 16, 0, 0);
        else k.w[i] = __builtin_amdgcn_raw_buffer_load_b128(rv, (id - 1296) * 16, 0, 0); }
}
DI void kv_job(lptr lds, const KvPre& k, int a, int lane, int mt, int s, const Params& P, u64 tag, unsigned cnt_v, unsigned one_v, int bid) {
    char* ws = P.ws; char* DBGWS = ws; (void)DBGWS;
    lptr cnt = lds + L_CNT;
    __builtin_amdgcn_s_setprio(KV_PRIO);
    const int hr = lane >> 5, c = lane & 31, rr = 2 * a + hr, lrow = 8 * s + rr;
    unsigned xv[9];
#pragma unroll
    for (int j = 0; j < 9; ++j) { const unsigned dw = c + 32 * j, ch = dw >> 2; xv[j] = *(LAS unsigned*)(lds + L_X + lrow * 1152 + (((ch & ~7u) | ((ch & 7u) ^ ((lrow >> 1) & 7u))) << 4) + 4 * (dw & 3)); }
    float sum = 0.f;
#pragma unroll
    for (int j = 0; j < 9; ++j) sum += blo(xv[j]) + bhi(xv[j]);
    const float mean = sum32(sum) * (1.f / DM);
    float sq = 0.f;
#pragma unroll
    for (int j = 0; j < 9; ++j) { const float d0 = blo(xv[j]) - mean, d1 = bhi(xv[j]) - mean; sq = __builtin_fmaf(d0, d0, sq); sq = __builtin_fmaf(d1, d1, sq); }
    const float mb = bfr(mean), sd = bfr(__builtin_amdgcn_sqrtf(bfr(bfr(sum32(sq) * (1.f / DM)) + 1e-5f))), rinv = __builtin_amdgcn_rcpf(sd);
#pragma unroll
    for (int j = 0; j < 9; ++j) xv[j] = cvtpk(bfr(blo(xv[j]) - mb) * rinv, bfr(bhi(xv[j]) - mb) * rinv);
    STAMP(33);
    BARRIER();
#pragma unroll
    for (int j = 0; j < 9; ++j) *(LAS unsigned*)(lds + L_KVX + rr * KV_XS + 4 * (c + 32 * j)) = xv[j];
    { const int t256 = a * 64 + lane;
#pragma unroll
      for (int i = 0; i < 11; ++i) { const int id = t256 + 256 * i; if (id < 2592) *(LAS u32x4*)(lds + L_H + (id < 1296 ? id * 16 : KV_WV + (id - 1296) * 16)) = k.w[i]; } }
    WAITL0();
    lds_add1(cnt_v + C_KV1, one_v);
    lds_wait_ge(cnt + C_KV1, 4);
    STAMP(36);
    const int i16 = lane & 15, g = lane >> 4;
    const int col = (a & 1) ? (16 + (i16 < 2 ? i16 : 1)) : i16;
    lptr wp = lds + L_H + (a < 2 ? 0u : KV_WV) + (8 * g) * 36 + 2 * col;
    lptr xp = lds + L_KVX + (i16 & 7) * KV_XS + 16 * g;
    f32x4 acc = {0.f, 0.f, 0.f, 0.f};
    {
        u32x4 wf[18], xq[18];
#pragma unroll
        for (int t = 0; t < 18; ++t) {
#pragma unroll
            for (int j = 0; j < 4; ++j) wf[t][j] = (unsigned)*(LAS u16*)(wp + t * 32 * 36 + (2 * j) * 36) | ((unsigned)*(LAS u16*)(wp + t * 32 * 36 + (2 * j + 1) * 36) << 16);
            xq[t] = *(LAS u32x4*)(xp + t * 64); }
#pragma unroll
        for (int t = 0; t < 18; ++t) { PIN(wf[t]); PIN(xq[t]); }
        if (a < 2) {
#pragma unroll
            for (int t = 0; t < 18; ++t) acc = MFMA16(wf[t], xq[t], acc);
        } else {
#pragma unroll
            for (int t = 0; t < 18; ++t) acc = MFMA16(xq[t], wf[t], acc);
        }
    }
    const __amdgpu_buffer_rsrc_t rs = mkrsrc(ws, WS_KV_END);
    const int ky0 = 128 * mt + 8 * s;
    const unsigned p01 = cvtpk(acc[0], acc[1]), p23 = cvtpk(acc[2], acc[3]);
    if (a == 0) { if (i16 < 8) { const int ky = ky0 + i16; __builtin_amdgcn_raw_buffer_store_b64((u32x2){p01, p23}, rs, WS_K1 + (ky >> 5) * 1024 + ((ky & 31) + 32 * (g & 1)) * 16 + 8 * (g >> 1), 0, SC1); } }
    else if (a == 1) { if (i16 < 8 && g == 0) { const int ky = ky0 + i16; __builtin_amdgcn_raw_buffer_store_b128((u32x4){p01, 0x00003F80u, 0u, 0u}, rs, WS_K2 + (ky >> 5) * 512 + (ky & 31) * 16, 0, SC1); } }
    else { const int cc = (a == 2 ? 0 : 16) + i16; const bool ones = (cc == 18);
        if (g < 2 && cc <= 18) __builtin_amdgcn_raw_buffer_store_b64(ones ? (u32x2){0x3F803F80u, 0x3F803F80u} : (u32x2){p01, p23}, rs, WS_V + (ky0 >> 4) * 608 + g * 304 + cc * 16 + 8 * (s & 1), 0, SC1); }
    __builtin_amdgcn_s_setprio(0);
    STAMP(37);
    WAITV(0);
    STAMP(38);
    lds_add1(cnt_v + C_KV2, one_v);
    if (a == 0) { lds_wait_ge(cnt + C_KV2, 4);
        if (lane < 8) gst64(ws + WS_FB + lane * 2048 + bid * 8, tag); }
}

struct Tgt { __amdgpu_buffer_rsrc_t rs[3]; unsigned base[3], ld[3], jstr; unsigned vo; };
struct Ffn {
    u32x4 xf[36];
    u32x4 ag[2], au[2];
    lptr rb0, rb1;
    Tgt cur, nxt;
    lptr lds; unsigned sync_target; unsigned cnt_v, one_v;
};
template <int Q> DI void ffn_dma(const Tgt& t, int j) { dma16(t.ld[Q] + j * SLOT, t.vo, t.rs[Q], t.base[Q] + j * t.jstr); }
template <int T, int U> DI void ffn_read1(Ffn& f) {
    constexpr int j = T / 6, kk = T % 6; lptr b = (j < 3 ? f.rb0 : f.rb1) + (j % 3) * SLOT + kk * 1024 + U * 6144;
    if constexpr (U) f.au[T % 2] = trfrag(b, 256); else f.ag[T % 2] = trfrag(b, 256);
}
template <int T> DI void ffn_issue(Ffn& f) {
    constexpr int j = T / 6, kk = T % 6;
    if constexpr (kk == 4 || kk == 0 || kk == 2) {
        constexpr int q = kk == 4 ? 0 : kk == 0 ? 1 : 2; constexpr int m = (kk == 4 ? j : j - 1) + 5;
        if constexpr (m < 6) ffn_dma<q>(f.cur, m); else ffn_dma<q>(f.nxt, m - 6);
    }
}
#define SYNC_ON true
template <int T> DI void ffn_norm(Ffn& f, f32x2 r2, f32x2 c2) {
    float lo[4], hi[4];
#pragma unroll
    for (int i = 0; i < 4; ++i) { lo[i] = blo(f.xf[T][i]); hi[i] = bhi(f.xf[T][i]); }
    SBAR();
    f32x2 t[4];
#pragma unroll
    for (int i = 0; i < 4; ++i) t[i] = __builtin_elementwise_fma((f32x2){lo[i], hi[i]}, r2, c2);
    SBAR();
#pragma unroll
    for (int i = 0; i < 4; ++i) f.xf[T][i] = cvtpk(t[i][0], t[i][1]);
    PIN(f.xf[T]);
}
template <bool FIRST, int T> DI void ffn_step(Ffn& f, f32x16& g, f32x16& u, unsigned& polled, f32x2 r2, f32x2 c2, const f32x16& pg, const f32x16& pu, u32x4& w0, u32x4& w1, lptr hp) {
    constexpr int kk = T % 6;
    if constexpr (SYNC_ON && kk == 1) {
        WAITV(8);
        lds_add1(f.cnt_v + C_RING, f.one_v);
        f.sync_target += 4;
    }
    if constexpr (SYNC_ON && kk == 3) polled = lds_ld(f.lds + L_CNT + C_RING);
    if constexpr (SYNC_ON && kk == 4) {
        if (__builtin_expect((unsigned)__builtin_amdgcn_readfirstlane(polled) < f.sync_target, 0)) {
            for (unsigned n = 0; (unsigned)__builtin_amdgcn_readfirstlane(lds_ld(f.lds + L_CNT + C_RING)) < f.sync_target && n < (1u << 22); ++n) __builtin_amdgcn_s_sleep(1); }
        FENCE();
    }
    ffn_read1<(T + 1) % 36, 0>(f);
    SBAR();
    if constexpr (T == 0) g = MFMA32(f.ag[0], f.xf[0], ZERO16); else g = MFMA32(f.ag[T % 2], f.xf[T], g);
    ffn_issue<T>(f);
    SBAR();
    ffn_read1<(T + 1) % 36, 1>(f);
    SBAR();
    if constexpr (T == 0) u = MFMA32(f.au[0], f.xf[0], ZERO16); else u = MFMA32(f.au[T % 2], f.xf[T], u);
    if constexpr (FIRST && T + 2 < 36) ffn_norm<T + 2>(f, r2, c2);
    if constexpr (!FIRST && T >= 2 && T < 10) { constexpr int i = T - 2;
        const unsigned v = cvtpk(__builtin_fmaxf(pg[2 * i], 0.f) * pu[2 * i], __builtin_fmaxf(pg[2 * i + 1], 0.f) * pu[2 * i + 1]);
        if constexpr (i < 4) { w0[i] = v; PIN(w0); } else { w1[i - 4] = v; PIN(w1); }
        if constexpr (i == 3) *(LAS u32x4*)hp = w0;
        if constexpr (i == 7) *(LAS u32x4*)(hp + 1024) = w1; }
    SBAR();
}
template <bool FIRST, int... Ts> DI void ffn_chunk_(Ffn& f, f32x16& g, f32x16& u, f32x2 r2, f32x2 c2, const f32x16& pg, const f32x16& pu, lptr hp, std::integer_sequence<int, Ts...>) { unsigned polled = 0; u32x4 w0, w1; (ffn_step<FIRST, Ts>(f, g, u, polled, r2, c2, pg, pu, w0, w1, hp), ...); }

DI void ffn_path(lptr lds, const Params& P, int mt, int s, int w, int lane) {
    char* DBGWS = P.ws; (void)DBGWS;
    Ffn f; f.lds = lds;
    const int h = lane >> 5, g4 = lane >> 4, q = (lane & 15) >> 2, p = lane & 3;
    const unsigned ring = (unsigned)(uintptr_t)(lds + L_RING);
    const __amdgpu_buffer_rsrc_t rsG = mkrsrc(P.wg, DM * FF * 2), rsU = mkrsrc(P.wu, DM * FF * 2), rs1 = mkrsrc(w < 2 ? P.wg : P.wu, DM * FF * 2);
    { const __amdgpu_buffer_rsrc_t rsQ = mkrsrc(P.wq, DM * DM * 2);
#pragma unroll
      for (int i = 0; i < 3; ++i) { const int pi = w + 4 * i; const bool up = pi >= 6; const int pr = up ? pi - 6 : pi; const unsigned row = 16u * pr;
          f.cur.ld[i] = ring + (up ? 6144u : 0u) + pr * 1024u; f.cur.rs[i] = rsQ; f.cur.base[i] = row * 1152u + (unsigned)s * 72u + (up ? 36u : 0u);
          f.nxt.ld[i] = f.cur.ld[i]; f.nxt.base[i] = row * 9216u + (unsigned)s * 576u; }
      f.cur.jstr = 96u * 1152u; f.cur.vo = (lane >> 2) * 1152 + (lane & 3) * 16;
      f.nxt.rs[0] = rsG; f.nxt.rs[1] = rs1; f.nxt.rs[2] = rsU; f.nxt.jstr = 96u * 9216u; f.nxt.vo = (lane >> 2) * 9216 + (lane & 3) * 16; }
    f.rb0 = lds + L_RING + (8 * h + q) * 64 + (g4 & 1) * 32 + 8 * p; { lptr t1 = f.rb0 + 3 * SLOT; PIN(t1); f.rb1 = t1; }
    { unsigned cv = (unsigned)(uintptr_t)(lds + L_CNT), ov = 1u; PIN(cv); PIN(ov); f.cnt_v = cv; f.one_v = ov; }
    f32x16 sA = {}, sB = {}; f32x2 r2, c2;
    {
        const int r = lane & 31; const unsigned gx = (r >> 1) & 7; lptr xb = lds + L_X + (32 * w + r) * 1152;
        lptr xb4[4];
#pragma unroll
        for (int i = 0; i < 4; ++i) xb4[i] = xb + 16 * ((2 * i + h) ^ gx);
#pragma unroll
        for (int t = 0; t < 36; ++t) f.xf[t] = *(LAS u32x4*)(xb4[t & 3] + 128 * (t >> 2));
    }
    const u32x4 ones = {0x3F803F80u, 0x3F803F80u, 0x3F803F80u, 0x3F803F80u};
#pragma unroll
    for (int t = 0; t < 16; ++t) { sA = MFMA32(ones, f.xf[t], sA); sB = MFMA32(f.xf[t], f.xf[t], sB); PIN(sA); PIN(sB); SBAR(); }
    BARRIER();
    STAMP(26);
#define STAT(t) do { sA = MFMA32(ones, f.xf[t], sA); sB = MFMA32(f.xf[t], f.xf[t], sB); PIN(sA); PIN(sB); SBAR(); } while (0)
    STAT(16); STAT(17); STAT(18);
    ffn_dma<0>(f.cur, 1); STAT(19); ffn_dma<1>(f.cur, 1); STAT(20); ffn_dma<2>(f.cur, 1); STAT(21);
    ffn_dma<0>(f.cur, 2); STAT(22); ffn_dma<1>(f.cur, 2); STAT(23); ffn_dma<2>(f.cur, 2); STAT(24);
    ffn_dma<0>(f.cur, 3); STAT(25); ffn_dma<1>(f.cur, 3); STAT(26); ffn_dma<2>(f.cur, 3); STAT(27);
    ffn_dma<0>(f.cur, 4); STAT(28); STAT(29); STAT(30); STAT(31); STAT(32); STAT(33); STAT(34); STAT(35);
#undef STAT
    {
        const float rowsum = sA[0];
        const int r = lane & 31, reg = (r & 3) + 4 * (r >> 3);
        float dg = sB[0];
#pragma unroll
        for (int i = 1; i < 16; ++i) dg = reg == i ? sB[i] : dg;
        dg = ((r >> 2) & 1) == h ? dg : 0.f;
        auto sw = __builtin_amdgcn_permlane32_swap(__float_as_uint(dg), __float_as_uint(dg), false, false);
        const float sq = __uint_as_float(sw[0]) + __uint_as_float(sw[1]);
        const float mean = rowsum * (1.f / DM); float var = fmaxf(sq * (1.f / DM) - mean * mean, 0.f);
        if (__builtin_expect(__any(mean * mean > 256.f * var), 0)) {
            float v2 = 0.f; const u32x4* xr = (const u32x4*)(P.x + (size_t)(128 * mt + 32 * w + r) * DM) + h;
#pragma unroll 1
            for (int t = 0; t < 36; ++t) { const u32x4 xv = xr[2 * t];
#pragma unroll
                for (int i = 0; i < 4; ++i) { const float d0 = blo(xv[i]) - mean, d1 = bhi(xv[i]) - mean; v2 = __builtin_fmaf(d0, d0, v2); v2 = __builtin_fmaf(d1, d1, v2); } }
            auto s2 = __builtin_amdgcn_permlane32_swap(__float_as_uint(v2), __float_as_uint(v2), false, false);
            var = (__uint_as_float(s2[0]) + __uint_as_float(s2[1])) * (1.f / DM);
        }
        const float mb = bfr(mean), sd = bfr(__builtin_amdgcn_sqrtf(bfr(bfr(var) + 1e-5f)));
        const float rinv = __builtin_amdgcn_rcpf(sd), cc = -mb * rinv; r2 = (f32x2){rinv, rinv}; c2 = (f32x2){cc, cc};
    }
    PIN(r2); PIN(c2);
#pragma unroll
    for (int t = 0; t < 36; ++t) PIN(f.xf[t]);
    ffn_norm<0>(f, r2, c2); ffn_norm<1>(f, r2, c2);
    STAMP(27);
    f.sync_target = 0;
    STAMP(2);
    ffn_read1<0, 0>(f); ffn_read1<0, 1>(f);
    __builtin_amdgcn_s_setprio(FFN_PRIO);
    auto next_params = [&](int c) {
        f.cur = f.nxt;
        if (c < 8) {
#pragma unroll
            for (int i = 0; i < 3; ++i) f.nxt.base[i] += 64u;
        } else if (c == 8) {
            const __amdgpu_buffer_rsrc_t rsD = mkrsrc(P.wd, FF * DM * 2);
#pragma unroll
            for (int i = 0; i < 3; ++i) { f.nxt.rs[i] = rsD; f.nxt.ld[i] = ring + 1024u * (w + 4 * i); f.nxt.base[i] = (288u * s + 8u * (w + 4 * i)) * 1152u; }
            f.nxt.jstr = 96u * 1152u; f.nxt.vo = (lane >> 3) * 1152 + (((lane & 7) ^ (((lane >> 4) & 1) << 2)) * 16);
        }
    };
    {
        f32x16 g, u;
        ffn_chunk_<true>(f, g, u, r2, c2, g, u, lds, std::make_integer_sequence<int, 36>{});
        const bool lo = lane < 32;
#pragma unroll
        for (int a = 0; a < 2; ++a) { const f32x16& qq = a ? u : g; u32x4 f0, f1;
#pragma unroll
            for (int i = 0; i < 4; ++i) f0[i] = cvtpk(qq[2 * i] * QSCALE, qq[2 * i + 1] * QSCALE);
            f1 = (u32x4){lo ? cvtpk(qq[8] * QSCALE, qq[9] * QSCALE) : 0u, 0u, 0u, 0u};
            *(LAS u32x4*)(lds + L_Q + (w * 2 + a) * 2048 + lane * 16) = f0;
            *(LAS u32x4*)(lds + L_Q + (w * 2 + a) * 2048 + 1024 + lane * 16) = f1; }
        WAITL0();
        lds_add1(f.cnt_v + C_Q, f.one_v);
        next_params(0);
        STAMP(3);
    }
    lptr hw = lds + L_H + w * 2048 + lane * 16;
    f32x16 gA, uA, gB = {}, uB = {};
    for (int c = 1; c < 10; c += 2) {
        if (c == 9) lds_wait_ge(lds + L_CNT + C_QTAKEN, 4);
        ffn_chunk_<false>(f, gA, uA, r2, c2, gB, uB, c == 1 ? lds + L_O + w * 2048 + lane * 16 : hw + (c - 2) * 8192, std::make_integer_sequence<int, 36>{});
        next_params(c);
        STAMP(3 + c);
        if (c + 1 < 10) {
            if (c == 1) lds_wait_ge(lds + L_CNT + C_KV2, 4);
            ffn_chunk_<false>(f, gB, uB, r2, c2, gA, uA, hw + (c - 1) * 8192, std::make_integer_sequence<int, 36>{});
            next_params(c + 1);
            STAMP(4 + c);
        }
    }
    {
        u32x4 w0, w1;
#pragma unroll
        for (int i = 0; i < 4; ++i) { w0[i] = cvtpk(__builtin_fmaxf(gA[2 * i], 0.f) * uA[2 * i], __builtin_fmaxf(gA[2 * i + 1], 0.f) * uA[2 * i + 1]);
                                      w1[i] = cvtpk(__builtin_fmaxf(gA[8 + 2 * i], 0.f) * uA[8 + 2 * i], __builtin_fmaxf(gA[9 + 2 * i], 0.f) * uA[9 + 2 * i]); }
        *(LAS u32x4*)(hw + 8 * 8192) = w0; *(LAS u32x4*)(hw + 8 * 8192 + 1024) = w1;
    }
    __builtin_amdgcn_s_setprio(0);
    WAITV(0);
}

struct KVf { u32x4 k1, k2, v0, v1; };
#define EX(x) __builtin_amdgcn_exp2f(x)
#define APRIO_HI() __builtin_amdgcn_s_setprio(ATT_TOGGLE)
#define APRIO_LO() __builtin_amdgcn_s_setprio(0)
DI void att_slot(f32x16& Sc, f32x16& Sn, u32x4& pup, const u32x4& k1, const u32x4& k2, const u32x4& q1, const u32x4& q2,
                 const u32x4& v1p, f32x16& Op, const u32x4& v0c, f32x16& Oc) {
    u32x4 plo;
    pup[2] = cvtpk(Sn[12], Sn[13]); pup[3] = cvtpk(Sn[14], Sn[15]); PIN(pup); SBAR();
    APRIO_HI(); Sn = MFMA32(k1, q1, ZERO16); SBAR(); APRIO_LO();
    Sc[0] = EX(Sc[0]); Sc[1] = EX(Sc[1]); Sc[2] = EX(Sc[2]); Sc[3] = EX(Sc[3]); PIN(Sc); SBAR();
    APRIO_HI(); Op = MFMA32(v1p, pup, Op); SBAR(); APRIO_LO();
    Sc[4] = EX(Sc[4]); Sc[5] = EX(Sc[5]); Sc[6] = EX(Sc[6]); Sc[7] = EX(Sc[7]); plo[0] = cvtpk(Sc[0], Sc[1]); plo[1] = cvtpk(Sc[2], Sc[3]); PIN(Sc); PIN(plo); SBAR();
    APRIO_HI(); Sn = MFMA32(k2, q2, Sn); SBAR(); APRIO_LO();
    Sc[8] = EX(Sc[8]); Sc[9] = EX(Sc[9]); Sc[10] = EX(Sc[10]); Sc[11] = EX(Sc[11]); plo[2] = cvtpk(Sc[4], Sc[5]); plo[3] = cvtpk(Sc[6], Sc[7]); PIN(Sc); PIN(plo); SBAR();
    Sc[12] = EX(Sc[12]); Sc[13] = EX(Sc[13]); SBAR();
    APRIO_HI(); Oc = MFMA32(v0c, plo, Oc); SBAR(); APRIO_LO();
    Sc[14] = EX(Sc[14]); Sc[15] = EX(Sc[15]); pup[0] = cvtpk(Sc[8], Sc[9]); pup[1] = cvtpk(Sc[10], Sc[11]); PIN(Sc); PIN(pup); SBAR();
}
DI void att_path(lptr lds, const Params& P, const KvPre& kpre, int mt, int s, int a4, int lane, u64 tag, int bid) {
    char* DBGWS = P.ws; (void)DBGWS;
    unsigned cnt_v = (unsigned)(uintptr_t)(lds + L_CNT), one_v = 1u; PIN(cnt_v); PIN(one_v);
    kv_job(lds, kpre, a4, lane, mt, s, P, tag, cnt_v, one_v, bid);
    STAMP(26);
    {
        const int s0 = s & ~(XMAP - 1); constexpr int nl = XMAP * 324 * 9, per = (nl + 8191) / 8192; unsigned sink = 0u;
#pragma unroll
        for (int i = 0; i < per; ++i) { const int id = ((bid >> 3) * per + i) * 256 + a4 * 64 + lane;
            if (id < nl) { const int sl = id / 2916, rem = id - 2916 * sl;
                const char* ptr = rem < 2592 ? (const char*)P.wd + (size_t)(288 * (s0 + sl)) * 1152 + rem * 128 : (const char*)P.wo + (size_t)(36 * (s0 + sl)) * 1152 + (rem - 2592) * 128;
                sink ^= *(const unsigned*)ptr; } }
        asm volatile("" :: "v"(sink));
    }
    if (a4 == 0) { wait_flags<4, KV_SLEEP>(P.ws + WS_FB + (bid & 7) * 2048, 64, tag, lane);
        lds_add1(cnt_v + C_KVOK, one_v);
        if (bid == 0 && lane == 0) gst64(P.ws + WS_G, tag); }
    else lds_wait_ge(lds + L_CNT + C_KVOK, 1);
    STAMP(27);
    const int rh = a4 & 1, kh = a4 >> 1, r = lane & 31, h = lane >> 5;
    const __amdgpu_buffer_rsrc_t rs = mkrsrc(P.ws, WS_KV_END);
    const unsigned voK1 = WS_K1 + lane * 16, voK2 = h == 0 ? WS_K2 + r * 16 : OOB, voV = r < 19 ? WS_V + h * 304 + r * 16 : OOB;
#define LDK(B, kb) do { B.k1 = __builtin_amdgcn_raw_buffer_load_b128(rs, voK1, (kb) * 1024, LDAUX); B.k2 = __builtin_amdgcn_raw_buffer_load_b128(rs, voK2, (kb) * 512, LDAUX); } while (0)
#define LDV(B, kb) do { B.v0 = __builtin_amdgcn_raw_buffer_load_b128(rs, voV, (kb) * 1216, LDAUX); B.v1 = __builtin_amdgcn_raw_buffer_load_b128(rs, voV, (kb) * 1216 + 608, LDAUX); } while (0)
    KVf A, B;
    const int kb0 = 32 * kh;
    KVf R; LDK(R, 0);
    LDK(A, kb0); LDV(A, kb0); LDK(B, kb0 + 1);
    lds_wait_ge(lds + L_CNT + C_Q, 4);
    STAMP(1);
    u32x4 q1[4], q2[4];
#pragma unroll
    for (int T = 0; T < 4; ++T) { const int rt = 2 * rh + (T >> 1), a = T & 1;
        q1[T] = *(LAS u32x4*)(lds + L_Q + (rt * 2 + a) * 2048 + lane * 16); q2[T] = *(LAS u32x4*)(lds + L_Q + (rt * 2 + a) * 2048 + 1024 + lane * 16); }
    WAITL0();
#pragma unroll
    for (int T = 0; T < 4; ++T) { PIN(q1[T]); PIN(q2[T]); }
    lds_add1(cnt_v + C_QTAKEN, one_v);
    float mref[4];
#pragma unroll
    for (int T = 0; T < 4; ++T) {
        f32x16 s0 = MFMA32(R.k1, q1[T], ZERO16); s0 = MFMA32(R.k2, q2[T], s0);
        float m = s0[0];
#pragma unroll
        for (int i = 1; i < 16; ++i) m = fmaxf(m, s0[i]);
        m = swap32_max(m); const unsigned mb = cvtpk(-m, 0.f) & 0xffffu; mref[T] = -__uint_as_float(mb << 16);
        if (h == 0) q2[T][1] = mb;
    }
    STAMP(2);
    f32x16 O[4] = {}; f32x16 S0, S1 = {}; u32x4 pup = {0u, 0u, 0u, 0u};
    B.v1 = A.v1;
    S0 = MFMA32(A.k1, q1[0], ZERO16); S0 = MFMA32(A.k2, q2[0], S0);
    for (int it = 0; it < 16; ++it) {
        const int kb = kb0 + 2 * it;
        att_slot(S0, S1, pup, A.k1, A.k2, q1[1], q2[1], B.v1, O[3], A.v0, O[0]);
        LDV(B, kb + 1);
        att_slot(S1, S0, pup, A.k1, A.k2, q1[2], q2[2], A.v1, O[0], A.v0, O[1]);
        att_slot(S0, S1, pup, A.k1, A.k2, q1[3], q2[3], A.v1, O[1], A.v0, O[2]);
        { const int kn = (kb + 2 < 64) ? kb + 2 : 63; LDK(A, kn); }
        att_slot(S1, S0, pup, B.k1, B.k2, q1[0], q2[0], A.v1, O[2], A.v0, O[3]);
        att_slot(S0, S1, pup, B.k1, B.k2, q1[1], q2[1], A.v1, O[3], B.v0, O[0]);
        { const int kn = (kb + 2 < 64) ? kb + 2 : 63; LDV(A, kn); }
        att_slot(S1, S0, pup, B.k1, B.k2, q1[2], q2[2], B.v1, O[0], B.v0, O[1]);
        att_slot(S0, S1, pup, B.k1, B.k2, q1[3], q2[3], B.v1, O[1], B.v0, O[2]);
        { const int kn = (kb + 3 < 64) ? kb + 3 : 63; LDK(B, kn); }
        att_slot(S1, S0, pup, A.k1, A.k2, q1[0], q2[0], B.v1, O[2], B.v0, O[3]);
    }
    pup[2] = cvtpk(S1[12], S1[13]); pup[3] = cvtpk(S1[14], S1[15]);
    O[3] = MFMA32(B.v1, pup, O[3]);
    {
        bool bad = false;
#pragma unroll
        for (int T = 0; T < 4; ++T) { float mx = 0.f;
#pragma unroll
            for (int i = 0; i < 10; ++i) mx = fmaxf(mx, __builtin_fabsf(O[T][i]));
            bad |= !(O[T][10] < 0x1p60f) || !(mx < 0x1p120f) ;
#pragma unroll
            for (int i = 0; i < 10; ++i) bad |= O[T][i] != O[T][i]; }
        if (__builtin_expect(__any(bad), 0)) {
#pragma unroll
            for (int T = 0; T < 4; ++T) { O[T] = ZERO16; mref[T] = -__builtin_inff(); if (h == 0) q2[T][1] = 0u; }
            for (int kb = kb0; kb < kb0 + 32; ++kb) {
                KVf C; LDK(C, kb); LDV(C, kb);
#pragma unroll
                for (int T = 0; T < 4; ++T) {
                    f32x16 sc = MFMA32(C.k1, q1[T], ZERO16); sc = MFMA32(C.k2, q2[T], sc);
                    float m = sc[0];
#pragma unroll
                    for (int i = 1; i < 16; ++i) m = fmaxf(m, sc[i]);
                    m = fmaxf(swap32_max(m), mref[T]);
                    const float al = EX(mref[T] - m); mref[T] = m;
#pragma unroll
                    for (int i = 0; i < 16; ++i) { O[T][i] *= al; sc[i] = EX(sc[i] - m); }
                    const u32x4 p0 = {cvtpk(sc[0], sc[1]), cvtpk(sc[2], sc[3]), cvtpk(sc[4], sc[5]), cvtpk(sc[6], sc[7])}, p1 = {cvtpk(sc[8], sc[9]), cvtpk(sc[10], sc[11]), cvtpk(sc[12], sc[13]), cvtpk(sc[14], sc[15])};
                    O[T] = MFMA32(C.v0, p0, O[T]); O[T] = MFMA32(C.v1, p1, O[T]);
                }
            }
        }
    }
    STAMP(9);
    BARRIER();
    {
        const __amdgpu_buffer_rsrc_t rsO = mkrsrc(P.wo, DM * DM * 2); const unsigned voD = (lane >> 3) * 1152 + (((lane & 7) ^ (((lane >> 4) & 1) << 2)) * 16), ring = (unsigned)(uintptr_t)(lds + L_RING);
        dma16(ring + (36 + a4) * 1024u, voD, rsO, (unsigned)(36 * s + 8 * a4) * 1152u);
        if (a4 < 2) dma16(ring + (40 + a4) * 1024u, voD, rsO, (unsigned)(36 * s + 8 * (a4 + 4)) * 1152u); }
    auto send = [&](const f32x16& o, float m, int rr) {
        lptr d = lds + L_XCH + (a4 * 2 + rr) * 3072 + lane * 16;
        *(LAS f32x4*)(d) = (f32x4){o[0], o[1], o[2], o[3]}; *(LAS f32x4*)(d + 1024) = (f32x4){o[4], o[5], o[6], o[7]}; *(LAS f32x4*)(d + 2048) = (f32x4){o[8], o[9], o[10], m}; };
    if (kh) { send(O[0], mref[0], 0); send(O[2], mref[2], 1); } else { send(O[1], mref[1], 0); send(O[3], mref[3], 1); }
    BARRIER();
    auto comb = [&](const f32x16& o, float m, int rr) {
        lptr d = lds + L_XCH + ((a4 ^ 2) * 2 + rr) * 3072 + lane * 16;
        const f32x4 a0 = *(LAS f32x4*)(d), a1 = *(LAS f32x4*)(d + 1024), a2 = *(LAS f32x4*)(d + 2048);
        const float M = fmaxf(m, a2[3]), fa = EX(m - M), fb = EX(a2[3] - M);
        float v[10];
#pragma unroll
        for (int i = 0; i < 4; ++i) { v[i] = o[i] * fa + a0[i] * fb; v[4 + i] = o[4 + i] * fa + a1[i] * fb; }
        v[8] = o[8] * fa + a2[0] * fb; v[9] = o[9] * fa + a2[1] * fb;
        float l = o[10] * fa + a2[2] * fb;
        l = __shfl(l, r, 64);
        const float il = __builtin_amdgcn_rcpf(l);
        lptr od = lds + L_O + (64 * rh + 32 * rr + r) * O_STRIDE + 36 * kh;
        *(LAS unsigned*)(od + 8 * h) = cvtpk(v[0] * il, v[1] * il); *(LAS unsigned*)(od + 8 * h + 4) = cvtpk(v[2] * il, v[3] * il);
        *(LAS unsigned*)(od + 16 + 8 * h) = cvtpk(v[4] * il, v[5] * il); *(LAS unsigned*)(od + 16 + 8 * h + 4) = cvtpk(v[6] * il, v[7] * il);
        if (h == 0) *(LAS unsigned*)(od + 32) = cvtpk(v[8] * il, v[9] * il);
        else if (kh == 1) { *(LAS unsigned*)(od + 36) = 0u; *(LAS unsigned*)(od + 40) = 0u; *(LAS unsigned*)(od + 44) = 0u; *(LAS unsigned*)(od + 48) = 0u; *(LAS unsigned*)(od + 52) = 0u; *(LAS unsigned*)(od + 56) = 0u; }
    };
    if (kh) { comb(O[1], mref[1], 0); comb(O[3], mref[3], 1); } else { comb(O[0], mref[0], 0); comb(O[2], mref[2], 1); }
}

struct PhB { __amdgpu_buffer_rsrc_t rsD, rsO, rsP; unsigned voD, vo5, ldsw, so0, so4, so5, po, d5, d5str; lptr tb0; u32x4 af[7];
             lptr tc, tn; unsigned vo_a, vo5_a, dst_a, d5_a, col_a, vo_b, vo5_b, dst_b, d5_b, col_b, pst, po_p; };
template <int NH, int I> DI void phb_piece(PhB& b, unsigned dst, unsigned d5, unsigned col, unsigned vo, unsigned vo5) {
    if constexpr (I < 4) dma16(dst + I * 8192, vo, b.rsD, b.so0 + I * (64u * 1152u) + col);
    else if constexpr (I == 4) { if constexpr (NH == 0) dma16(dst + I * 8192, vo, b.rsD, b.so4 + col); else dma16(dst + I * 8192, vo, b.rsO, b.so4 + col); }
    else if constexpr (NH == 0) dma16(d5, vo5, b.rsO, b.so5 + col);
}
template <int NH, int KT> DI void phb_issue(PhB& b) {
    constexpr int o = KT >= 17 ? KT - 17 : KT + 4;
    if constexpr (o < 18 && (o % 3) == (NH ? 2 : 0)) { if constexpr (KT >= 17) phb_piece<NH, o / 3>(b, b.dst_b, b.d5_b, b.col_b, b.vo_b, b.vo5_b); else phb_piece<NH, o / 3>(b, b.dst_a, b.d5_a, b.col_a, b.vo_a, b.vo5_a); }
}
template <int NH, int KT> DI void phb_step(PhB& b, const u32x4 (&hf)[21], f32x16& acc, f32x16& prev, u32x4& w0, u32x4& w1) {
    constexpr int P = NH ? 5 : 6;
    if constexpr (KT == 17) asm volatile("s_waitcnt vmcnt(%0) lgkmcnt(0)\n\ts_barrier" :: "i"(P + 2) : "memory");
    if constexpr (KT + 4 < 21) b.af[(KT + 4) % 7] = trfrag(b.tc + (KT + 4) * 2048, 1024); else b.af[(KT + 4) % 7] = trfrag(b.tn + (KT + 4 - 21) * 2048, 1024);
    SBAR();
    if constexpr (KT == 0) acc = MFMA32(b.af[0], hf[0], ZERO16); else acc = MFMA32(b.af[KT % 7], hf[KT], acc);
    phb_issue<NH, KT>(b);
    if constexpr (KT >= 2 && KT < 6) { constexpr int i = KT - 2;
        if constexpr (i < 2) { w0[2 * i] = cvtpk(prev[4 * i], prev[4 * i + 1]); w0[2 * i + 1] = cvtpk(prev[4 * i + 2], prev[4 * i + 3]); PIN(w0); }
        else { w1[2 * i - 4] = cvtpk(prev[4 * i], prev[4 * i + 1]); w1[2 * i - 3] = cvtpk(prev[4 * i + 2], prev[4 * i + 3]); PIN(w1); }
        if constexpr (i == 1) __builtin_amdgcn_raw_buffer_store_b128(w0, b.rsP, b.po_p, b.pst, SC1);
        if constexpr (i == 3) __builtin_amdgcn_raw_buffer_store_b128(w1, b.rsP, b.po_p + 1024, b.pst, SC1);
    }
    SBAR();
}
template <int NH, int... Ks> DI void phb_stage_(PhB& b, const u32x4 (&hf)[21], f32x16& acc, f32x16& prev, std::integer_sequence<int, Ks...>) {
    u32x4 w0, w1; (phb_step<NH, Ks>(b, hf, acc, prev, w0, w1), ...);
}
template <int NH> DI void phb_stage(PhB& b, const u32x4 (&hf)[21], f32x16& acc, f32x16& prev, int np) {
    const unsigned b0 = (unsigned)(np % 3) * BBUF, b1 = (unsigned)((np + 1) % 3) * BBUF, b2 = (unsigned)((np + 2) % 3) * BBUF;
    b.tc = b.tb0 + b0; b.tn = b.tb0 + b1;
    b.dst_a = b.ldsw + b2; b.d5_a = b.d5 + ((np + 2) % 3) * b.d5str; b.col_a = (np + 2) * 128u; b.vo_a = np + 2 < 9 ? b.voD : OOB; b.vo5_a = np + 2 < 9 ? b.vo5 : OOB;
    b.dst_b = b.ldsw + b0; b.d5_b = b.d5 + (np % 3) * b.d5str; b.col_b = (np + 3) * 128u; b.vo_b = np + 3 < 9 ? b.voD : OOB; b.vo5_b = np + 3 < 9 ? b.vo5 : OOB;
    b.pst = np > 0 ? (unsigned)(np - 1) * 4096u : 0u; b.po_p = np > 0 ? b.po : OOB;
    phb_stage_<NH>(b, hf, acc, prev, std::make_integer_sequence<int, 21>{});
}
template <int NH> DI void phb_run(PhB& b, const u32x4 (&hf)[21], bool early, char* eflag, u64 tag, char* DBGWS) {
    b.af[0] = trfrag(b.tb0, 1024); b.af[1] = trfrag(b.tb0 + 2048, 1024); b.af[2] = trfrag(b.tb0 + 4096, 1024); b.af[3] = trfrag(b.tb0 + 6144, 1024);
    phb_piece<NH, 0>(b, b.ldsw + BBUF, b.d5 + b.d5str, 128u, b.voD, b.vo5); phb_piece<NH, 1>(b, b.ldsw + BBUF, b.d5 + b.d5str, 128u, b.voD, b.vo5); phb_piece<NH, 2>(b, b.ldsw + BBUF, b.d5 + b.d5str, 128u, b.voD, b.vo5);
    phb_piece<NH, 3>(b, b.ldsw + BBUF, b.d5 + b.d5str, 128u, b.voD, b.vo5); phb_piece<NH, 4>(b, b.ldsw + BBUF, b.d5 + b.d5str, 128u, b.voD, b.vo5); phb_piece<NH, 5>(b, b.ldsw + BBUF, b.d5 + b.d5str, 128u, b.voD, b.vo5);
    phb_piece<NH, 0>(b, b.ldsw + 2 * BBUF, b.d5 + 2 * b.d5str, 256u, b.voD, b.vo5);
    if constexpr (NH == 0) phb_piece<NH, 1>(b, b.ldsw + 2 * BBUF, b.d5 + 2 * b.d5str, 256u, b.voD, b.vo5);
    f32x16 a0 = {}, a1 = {};
    for (int np = 0; np < 9; np += 2) {
        phb_stage<NH>(b, hf, a0, a1, np);
        STAMP(13 + np);
        if (np + 1 < 9) phb_stage<NH>(b, hf, a1, a0, np + 1);
        if (np == 6 && early) gst64(eflag, tag);
        STAMP(14 + np);
    }
    u32x4 w0, w1;
#pragma unroll
    for (int i = 0; i < 4; ++i) { w0[i] = cvtpk(a0[2 * i], a0[2 * i + 1]); w1[i] = cvtpk(a0[8 + 2 * i], a0[9 + 2 * i]); }
    __builtin_amdgcn_raw_buffer_store_b128(w0, b.rsP, b.po, 8 * 4096, SC1); __builtin_amdgcn_raw_buffer_store_b128(w1, b.rsP, b.po + 1024, 8 * 4096, SC1);
}

__global__ __launch_bounds__(512, 2) void fused_kernel(Params P) {
    extern __shared__ __attribute__((aligned(16))) char smem[];
    lptr lds = (lptr)smem;
    const int tid = threadIdx.x, lane0 = tid & 63, wid = __builtin_amdgcn_readfirstlane(tid >> 6);
    const int bid = blockIdx.x;
    const int xg = bid & 7, jj = bid >> 3, s = 4 * (xg & 3) + (jj & 3), mt = 8 * (xg >> 2) + (jj >> 2);
    char* DBGWS = P.ws; (void)DBGWS;
    STAMPR(30); STAMP(0);
    STAMP(46);
    const u64 g0 = gld64(P.ws + WS_G);
    if (tid < 16) *(LAS unsigned*)(lds + L_CNT + 4 * tid) = 0u;
#define CODEPF 360
    unsigned codepf = 0u;
    {
        const char* pc = (const char*)__builtin_amdgcn_s_getpc();
        if (tid < CODEPF) codepf = *(const GAS unsigned*)(pc + tid * 128); }
    {
        const __amdgpu_buffer_rsrc_t rsX = mkrsrc(P.x, SEQ * DM * 2);
        const unsigned ldsu0 = (unsigned)(uintptr_t)(lds + L_X) + wid * 18432u, so = (unsigned)(128 * mt + 16 * wid) * 1152u;
        unsigned row = 0u, cp = lane0;
#pragma unroll 2
        for (int i = 0; i < 18; ++i) {
            dma16(ldsu0 + i * 1024, row * 1152u + 16u * ((cp & ~7u) | ((cp & 7u) ^ ((row >> 1) & 7u))), rsX, so);
            cp += 64u; if (cp >= 72u) { cp -= 72u; row += 1u; } }
    }
    if (wid >= 4) {
        const int w = wid & 3; const __amdgpu_buffer_rsrc_t rsQ = mkrsrc(P.wq, DM * DM * 2);
        const unsigned vq = (lane0 >> 2) * 1152 + (lane0 & 3) * 16, ring = (unsigned)(uintptr_t)(lds + L_RING);
#pragma unroll
        for (int i = 0; i < 3; ++i) { const int pi = w + 4 * i; const bool up = pi >= 6; const int pr = up ? pi - 6 : pi;
            dma16(ring + (up ? 6144u : 0u) + pr * 1024u, vq, rsQ, 16u * pr * 1152u + (unsigned)s * 72u + (up ? 36u : 0u)); }
    }
    STAMP(47);
    KvPre kpre;
    if (wid < 4) kv_preload(kpre, tid, P);
    const u64 tag = g0 + 1ull;
    STAMP(32);
    if (wid < 4) WAITV(11); else WAITV(0);
    asm volatile("" :: "v"(codepf));
    BARRIER();
    STAMP(34);
    u32x4 hf[21];
    if (wid >= 4) { ffn_path(lds, P, mt, s, wid & 3, lane0); BARRIER();
#pragma unroll
        for (int t = 0; t < 18; ++t) hf[t] = *(LAS u32x4*)(lds + L_H + ((t >> 1) * 4 + (wid & 3)) * 2048 + (t & 1) * 1024 + lane0 * 16);
        BARRIER(); }
    else att_path(lds, P, kpre, mt, s, wid & 3, lane0, tag, bid);
    int tz = threadIdx.x; asm volatile("" : "+v"(tz));
    const int lane = tz & 63;
    const int rt = wid & 3, nh = wid >> 2, r = lane & 31, h = lane >> 5, g4 = lane >> 4, q = (lane & 15) >> 2, p = lane & 3;
    PhB b;
    b.rsD = mkrsrc(P.wd, FF * DM * 2); b.rsO = mkrsrc(P.wo, DM * DM * 2);
    b.voD = (lane >> 3) * 1152 + (((lane & 7) ^ (((lane >> 4) & 1) << 2)) * 16); b.vo5 = wid < 2 ? b.voD : OOB;
    b.ldsw = (unsigned)(uintptr_t)(lds + L_RING) + wid * 1024;
    b.d5 = wid < 2 ? b.ldsw + 5 * 8192 : (unsigned)(uintptr_t)(lds + L_RING) + 3 * BBUF; b.d5str = wid < 2 ? BBUF : 0u;
    b.so0 = (unsigned)(288 * s + 8 * wid) * 1152u;
    b.so4 = wid < 4 ? (unsigned)(288 * s + 8 * (wid + 32)) * 1152u : (unsigned)(36 * s + 8 * (wid - 4)) * 1152u;
    b.so5 = (unsigned)(36 * s + 8 * (wid + 4)) * 1152u;
    const unsigned rowl = 4 * h + q; const unsigned ch = (4 * nh + 2 * (g4 & 1) + (p >> 1)) ^ (((rowl >> 1) & 1) << 2);
    b.tb0 = lds + L_RING + rowl * 128 + ch * 16 + 8 * (p & 1);
    b.rsP = mkrsrc(P.ws + WS_PART + (size_t)(mt * 16 + s) * SLAB, SLAB);
    b.po = (unsigned)(rt * 18 + nh) * 2048u + lane * 16;
    BARRIER();
    STAMP(23);
    if (wid < 4) {
#pragma unroll
        for (int t = 0; t < 18; ++t) hf[t] = *(LAS u32x4*)(lds + L_H + ((t >> 1) * 4 + rt) * 2048 + (t & 1) * 1024 + lane * 16); }
#pragma unroll
    for (int t = 0; t < 3; ++t) { lptr o = lds + L_O + (32 * rt + r) * O_STRIDE + 32 * t + 8 * h; const u32x2 a = *(LAS u32x2*)o, bb = *(LAS u32x2*)(o + 16); hf[18 + t] = (u32x4){a[0], a[1], bb[0], bb[1]}; }
    WAITL0();
#pragma unroll
    for (int t = 0; t < 21; ++t) PIN(hf[t]);
    WAITV(0);
    BARRIER();
    STAMP(24);
    { const bool early = wid == 0 && lane == 0; char* ef = P.ws + WS_FE + mt * 128 + s * 8;
      if (nh) phb_run<1>(b, hf, early, ef, tag, DBGWS); else phb_run<0>(b, hf, early, ef, tag, DBGWS); }
    STAMP(25);
    const int urt = s >> 2, unh = (s >> 1) & 1, uhf = s & 1;
    const unsigned uo0 = (unsigned)(urt * 18 + 2 * wid + unh) * 2048u + uhf * 1024u, uo8 = (unsigned)(urt * 18 + 16 + unh) * 2048u + uhf * 1024u;
    const size_t ro0 = (size_t)(128 * mt + 32 * urt + r) * DM + 32 * (2 * wid + unh) + 16 * uhf + 4 * h;
    const u32x2 x0 = *(const u32x2*)(P.x + ro0), x1 = *(const u32x2*)(P.x + ro0 + 8);
    const int l8 = 8 * wid + (lane & 7);
    const size_t ro8 = (size_t)(128 * mt + 32 * urt + (l8 & 31)) * DM + 32 * (16 + unh) + 16 * uhf + 4 * (l8 >> 5);
    u32x2 y0 = {0u, 0u}, y1 = {0u, 0u};
    if (lane < 8) { y0 = *(const u32x2*)(P.x + ro8); y1 = *(const u32x2*)(P.x + ro8 + 8); }
    WAITV(0);
    STAMP(43);
    BARRIER();
    STAMP(44); STAMPR(45);
    if (wid == 0 && lane == 0) gst64(P.ws + WS_FC + mt * 128 + s * 8, tag);
    {   const __amdgpu_buffer_rsrc_t rp = mkrsrc(P.ws + WS_PART + (size_t)mt * 16 * SLAB, 16 * SLAB);
        u32x4 v[16], v8[16]; const unsigned vo8 = lane < 8 ? l8 * 16u : OOB;
        if (wid == 0) wait_flags<1, 2>(P.ws + WS_FE + mt * 128, 16, tag, lane);
        BARRIER();
        if (wid < 6) {
#pragma unroll
            for (int k = 0; k < 16; ++k) v[k] = __builtin_amdgcn_raw_buffer_load_b128(rp, lane * 16, k * SLAB + uo0, SC1); }
        if (wid == 7) wait_flags<1, 2>(P.ws + WS_FC + mt * 128, 16, tag, lane);
        BARRIER();
        STAMP(28);
        if (wid >= 6) {
#pragma unroll
            for (int k = 0; k < 16; ++k) v[k] = __builtin_amdgcn_raw_buffer_load_b128(rp, lane * 16, k * SLAB + uo0, SC1); }
#pragma unroll
        for (int k = 0; k < 16; ++k) v8[k] = __builtin_amdgcn_raw_buffer_load_b128(rp, vo8, k * SLAB + uo8, SC1);
        auto fin = [&](const u32x4 (&vv)[16], const u32x2& xa, const u32x2& xb, size_t ro, bool on) {
            float acc[8] = {blo(xa[0]), bhi(xa[0]), blo(xa[1]), bhi(xa[1]), blo(xb[0]), bhi(xb[0]), blo(xb[1]), bhi(xb[1])};
#pragma unroll
            for (int k = 0; k < 16; ++k)
#pragma unroll
                for (int i = 0; i < 4; ++i) { acc[2 * i] += blo(vv[k][i]); acc[2 * i + 1] += bhi(vv[k][i]); }
            if (on) { *(u32x2*)(P.out + ro) = (u32x2){cvtpk(acc[0], acc[1]), cvtpk(acc[2], acc[3])};
                      *(u32x2*)(P.out + ro + 8) = (u32x2){cvtpk(acc[4], acc[5]), cvtpk(acc[6], acc[7])}; } };
        fin(v, x0, x1, ro0, true);
        fin(v8, y0, y1, ro8, lane < 8);
    }
    STAMP(29); STAMPR(31);
}

extern "C" void kernel_launch(void* const* d_in, const int* in_sizes, int n_in, void* d_out, int out_size, void* d_ws, size_t ws_size, hipStream_t stream) {
    Params P; P.x = (const u16*)d_in[0]; P.wq = (const u16*)d_in[1]; P.wk = (const u16*)d_in[2]; P.wv = (const u16*)d_in[3]; P.wo = (const u16*)d_in[4];
    P.wg = (const u16*)d_in[6]; P.wu = (const u16*)d_in[7]; P.wd = (const u16*)d_in[8]; P.out = (u16*)d_out; P.ws = (char*)d_ws;
    (void)hipFuncSetAttribute((const void*)fused_kernel, hipFuncAttributeMaxDynamicSharedMemorySize, LDS_BYTES);
    fused_kernel<<<256, 512, LDS_BYTES, stream>>>(P);
}
```

```cpp
#include <hip/hip_runtime.h>
#include <stdint.h>
#include <utility>

#define XMAP 4
#define FFN_PRIO 1
#define ATT_TOGGLE 2
#define KV_PRIO 3
#define LDAUX 16
#define POLL_SLEEP 8
#define KV_SLEEP 8
#define CODEPF 416
#define DI __device__ __forceinline__
#define LAS __attribute__((address_space(3)))
#define GAS __attribute__((address_space(1)))
typedef unsigned short u16;
typedef unsigned long long u64;
typedef __bf16 bf16x8 __attribute__((ext_vector_type(8)));
typedef __bf16 bf16x2 __attribute__((ext_vector_type(2)));
typedef short s16x4 __attribute__((ext_vector_type(4)));
typedef float f32x2 __attribute__((ext_vector_type(2)));
typedef float f32x4 __attribute__((ext_vector_type(4)));
typedef float f32x16 __attribute__((ext_vector_type(16)));
typedef unsigned u32x2 __attribute__((ext_vector_type(2)));
typedef unsigned u32x4 __attribute__((ext_vector_type(4)));
typedef LAS char* lptr;

constexpr int SEQ = 2048, DM = 576, FF = 4608;
constexpr unsigned WS_K1 = 0, WS_K2 = 65536, WS_V = WS_K2 + 64 * 512, WS_KV_END = WS_V + 128 * 608;
constexpr unsigned WS_G = 3u << 20, WS_FB = WS_G + 8192, WS_FC = WS_FB + 16384, WS_FE = WS_FC + 4096;
constexpr size_t WS_PART = 4u << 20;
constexpr unsigned SLAB = 4 * 18 * 2048;
constexpr unsigned SLOT = 12288, L_CNT = 0, L_RING = 4096, L_H = L_RING + 6 * SLOT, L_Q = L_H + 57344, L_O = L_H + 73728, O_STRIDE = 96, L_XCH = L_RING + 4 * SLOT, LDS_BYTES = 163840;
constexpr unsigned BBUF = 43008;
constexpr unsigned L_X = 16384, L_KVX = L_H + 43008, KV_XS = 1168, KV_LDS = 43008 + 8 * 1168;
constexpr unsigned C_RING = 0, C_Q = 4, C_QTAKEN = 8, C_KV1 = 12, C_KV2 = 16, C_KVOK = 20;
constexpr unsigned OOB = 0x40000000u;
constexpr int SC1 = 16;
constexpr float QSCALE = 0.23570226039551584f * 1.4426950408889634f;

#define SBAR() __builtin_amdgcn_sched_barrier(0)
#define PIN(x) asm volatile("" : "+v"(x))
#define MFMA32(a, b, c) __builtin_amdgcn_mfma_f32_32x32x16_bf16(__builtin_bit_cast(bf16x8, a), __builtin_bit_cast(bf16x8, b), c, 0, 0, 0)
#define MFMA16(a, b, c) __builtin_amdgcn_mfma_f32_16x16x32_bf16(__builtin_bit_cast(bf16x8, a), __builtin_bit_cast(bf16x8, b), c, 0, 0, 0)
#define WAITV(n) asm volatile("s_waitcnt vmcnt(" #n ")" ::: "memory")
#define WAITL0() asm volatile("s_waitcnt lgkmcnt(0)" ::: "memory")
#define FENCE() asm volatile("" ::: "memory")
#define BARRIER() asm volatile("s_waitcnt lgkmcnt(0)\n\ts_barrier" ::: "memory")
#define ZERO16 ((f32x16){})
#define RLX_AGENT __ATOMIC_RELAXED, __HIP_MEMORY_SCOPE_AGENT

DI unsigned cvtpk(float lo, float hi) { f32x2 v = {lo, hi}; return __builtin_bit_cast(unsigned, __builtin_convertvector(v, bf16x2)); }
DI float bfr(float f) { return __uint_as_float(cvtpk(f, 0.f) << 16); }
DI float blo(unsigned v) { return __uint_as_float(v << 16); }
DI float bhi(unsigned v) { return __uint_as_float(v & 0xffff0000u); }
DI u32x2 trread(lptr p) { return __builtin_bit_cast(u32x2, __builtin_amdgcn_ds_read_tr16_b64_v4i16((LAS s16x4*)p)); }
DI u32x4 trfrag(lptr p, unsigned second) { u32x2 a = trread(p), b = trread(p + second); return (u32x4){a[0], a[1], b[0], b[1]}; }
DI __amdgpu_buffer_rsrc_t mkrsrc(const void* p, unsigned bytes) { return __builtin_amdgcn_make_buffer_rsrc((void*)p, (short)0, (int)bytes, 0x00020000); }
DI void dma16(unsigned ldsaddr, unsigned voff, __amdgpu_buffer_rsrc_t rs, unsigned soff) {
    asm volatile("s_mov_b32 m0, %0\n\ts_nop 0\n\tbuffer_load_dwordx4 %1, %2, %3 offen lds" :: "s"(ldsaddr), "v"(voff), "s"(rs), "s"(soff) : "memory");
}
DI float swap32_max(float v) { auto r = __builtin_amdgcn_permlane32_swap(__float_as_uint(v), __float_as_uint(v), false, false); return fmaxf(__uint_as_float(r[0]), __uint_as_float(r[1])); }
DI void lds_add1(unsigned addr_v, unsigned one_v) { asm volatile("s_mov_b64 exec, 1\n\tds_add_u32 %0, %1\n\ts_mov_b64 exec, -1" :: "v"(addr_v), "v"(one_v) : "memory"); }
DI unsigned lds_ld(lptr p) { return __hip_atomic_load((LAS unsigned*)p, __ATOMIC_RELAXED, __HIP_MEMORY_SCOPE_WORKGROUP); }
DI void lds_wait_ge(lptr p, unsigned v) { for (unsigned n = 0; (unsigned)__builtin_amdgcn_readfirstlane(lds_ld(p)) < v && n < (1u << 22); ++n) __builtin_amdgcn_s_sleep(1); FENCE(); }
DI u64 gld64(const char* p) { return __hip_atomic_load((GAS u64*)p, RLX_AGENT); }
DI void gst64(char* p, u64 v) { __hip_atomic_store((GAS u64*)p, v, RLX_AGENT); }
template <int PER_LANE, int SLEEP = POLL_SLEEP> DI void wait_flags(const char* base, int nlanes, u64 tag, int lane) {
    for (unsigned n = 0; n < (1u << 18); ++n) { bool ok = true;
        if (lane < nlanes) {
#pragma unroll
            for (int i = 0; i < PER_LANE; ++i) ok &= gld64(base + (lane + 64 * i) * 8) == tag; }
        if (__all(ok)) break;
        __builtin_amdgcn_s_sleep(SLEEP); }
    FENCE();
}


struct Params { const u16* x; const u16* wq; const u16* wk; const u16* wv; const u16* wo; const u16* wg; const u16* wu; const u16* wd; u16* out; char* ws; };

template <int CTRL> DI float dpp_add(float v) { return v + __uint_as_float(__builtin_amdgcn_update_dpp(0u, __float_as_uint(v), CTRL, 0xf, 0xf, true)); }
DI float sum32(float v) {
    v = dpp_add<0xB1>(v); v = dpp_add<0x4E>(v); v = dpp_add<0x141>(v); v = dpp_add<0x140>(v);
    auto r = __builtin_amdgcn_permlane16_swap(__float_as_uint(v), __float_as_uint(v), false, false); return __uint_as_float(r[0]) + __uint_as_float(r[1]);
}
constexpr unsigned KV_WV = 21 * 1024;
struct KvPre { u32x4 w[11]; };
DI void kv_preload(KvPre& k, int t256, const Params& P) {
    const __amdgpu_buffer_rsrc_t rk = mkrsrc(P.wk, DM * 18 * 2), rv = mkrsrc(P.wv, DM * 18 * 2);
#pragma unroll
    for (int i = 0; i < 11; ++i) { const int id = t256 + 256 * i;
        if (i < 5) k.w[i] = __builtin_amdgcn_raw_buffer_load_b128(rk, id * 16, 0, 0);
        else if (i == 5) k.w[i] = id < 1296 ? __builtin_amdgcn_raw_buffer_load_b128(rk, id * 16, 0, 0) : __builtin_amdgcn_raw_buffer_load_b128(rv, (id - 1296) * 16, 0, 0);
        else k.w[i] = __builtin_amdgcn_raw_buffer_load_b128(rv, (id - 1296) * 16, 0, 0); }
}
DI void kv_job(lptr lds, const KvPre& k, int a, int lane, int mt, int s, const Params& P, u64 tag, unsigned cnt_v, unsigned one_v, int bid) {
    char* ws = P.ws;
    lptr cnt = lds + L_CNT;
    __builtin_amdgcn_s_setprio(KV_PRIO);
    const int hr = lane >> 5, c = lane & 31, rr = 2 * a + hr, lrow = 8 * s + rr;
    unsigned xv[9];
#pragma unroll
    for (int j = 0; j < 9; ++j) { const unsigned dw = c + 32 * j, ch = dw >> 2; xv[j] = *(LAS unsigned*)(lds + L_X + lrow * 1152 + (((ch & ~7u) | ((ch & 7u) ^ ((lrow >> 1) & 7u))) << 4) + 4 * (dw & 3)); }
    float sum = 0.f;
#pragma unroll
    for (int j = 0; j < 9; ++j) sum += blo(xv[j]) + bhi(xv[j]);
    const float mean = sum32(sum) * (1.f / DM);
    float sq = 0.f;
#pragma unroll
    for (int j = 0; j < 9; ++j) { const float d0 = blo(xv[j]) - mean, d1 = bhi(xv[j]) - mean; sq = __builtin_fmaf(d0, d0, sq); sq = __builtin_fmaf(d1, d1, sq); }
    const float mb = bfr(mean), sd = bfr(__builtin_amdgcn_sqrtf(bfr(bfr(sum32(sq) * (1.f / DM)) + 1e-5f))), rinv = __builtin_amdgcn_rcpf(sd);
#pragma unroll
    for (int j = 0; j < 9; ++j) xv[j] = cvtpk(bfr(blo(xv[j]) - mb) * rinv, bfr(bhi(xv[j]) - mb) * rinv);
    BARRIER();
#pragma unroll
    for (int j = 0; j < 9; ++j) *(LAS unsigned*)(lds + L_KVX + rr * KV_XS + 4 * (c + 32 * j)) = xv[j];
    { const int t256 = a * 64 + lane;
#pragma unroll
      for (int i = 0; i < 11; ++i) { const int id = t256 + 256 * i; if (id < 2592) *(LAS u32x4*)(lds + L_H + (id < 1296 ? id * 16 : KV_WV + (id - 1296) * 16)) = k.w[i]; } }
    WAITL0();
    lds_add1(cnt_v + C_KV1, one_v);
    lds_wait_ge(cnt + C_KV1, 4);
    const int i16 = lane & 15, g = lane >> 4;
    const int col = (a & 1) ? (16 + (i16 < 2 ? i16 : 1)) : i16;
    lptr wp = lds + L_H + (a < 2 ? 0u : KV_WV) + (8 * g) * 36 + 2 * col;
    lptr xp = lds + L_KVX + (i16 & 7) * KV_XS + 16 * g;
    f32x4 acc = {0.f, 0.f, 0.f, 0.f};
    {
        u32x4 wf[18], xq[18];
#pragma unroll
        for (int t = 0; t < 18; ++t) {
#pragma unroll
            for (int j = 0; j < 4; ++j) wf[t][j] = (unsigned)*(LAS u16*)(wp + t * 32 * 36 + (2 * j) * 36) | ((unsigned)*(LAS u16*)(wp + t * 32 * 36 + (2 * j + 1) * 36) << 16);
            xq[t] = *(LAS u32x4*)(xp + t * 64); }
#pragma unroll
        for (int t = 0; t < 18; ++t) { PIN(wf[t]); PIN(xq[t]); }
        if (a < 2) {
#pragma unroll
            for (int t = 0; t < 18; ++t) acc = MFMA16(wf[t], xq[t], acc);
        } else {
#pragma unroll
            for (int t = 0; t < 18; ++t) acc = MFMA16(xq[t], wf[t], acc);
        }
    }
    const __amdgpu_buffer_rsrc_t rs = mkrsrc(ws, WS_KV_END);
    const int ky0 = 128 * mt + 8 * s;
    const unsigned p01 = cvtpk(acc[0], acc[1]), p23 = cvtpk(acc[2], acc[3]);
    if (a == 0) { if (i16 < 8) { const int ky = ky0 + i16; __builtin_amdgcn_raw_buffer_store_b64((u32x2){p01, p23}, rs, WS_K1 + (ky >> 5) * 1024 + ((ky & 31) + 32 * (g & 1)) * 16 + 8 * (g >> 1), 0, SC1); } }
    else if (a == 1) { if (i16 < 8 && g == 0) { const int ky = ky0 + i16; __builtin_amdgcn_raw_buffer_store_b128((u32x4){p01, 0x00003F80u, 0u, 0u}, rs, WS_K2 + (ky >> 5) * 512 + (ky & 31) * 16, 0, SC1); } }
    else { const int cc = (a == 2 ? 0 : 16) + i16; const bool ones = (cc == 18);
        if (g < 2 && cc <= 18) __builtin_amdgcn_raw_buffer_store_b64(ones ? (u32x2){0x3F803F80u, 0x3F803F80u} : (u32x2){p01, p23}, rs, WS_V + (ky0 >> 4) * 608 + g * 304 + cc * 16 + 8 * (s & 1), 0, SC1); }
    __builtin_amdgcn_s_setprio(0);
    WAITV(0);
    lds_add1(cnt_v + C_KV2, one_v);
    if (a == 0) { lds_wait_ge(cnt + C_KV2, 4);
        if (lane < 8) gst64(ws + WS_FB + lane * 2048 + bid * 8, tag); }
}

struct Tgt { __amdgpu_buffer_rsrc_t rs[3]; unsigned base[3], ld[3], jstr; unsigned vo; };
struct Ffn {
    u32x4 xf[36];
    u32x4 ag[2], au[2];
    lptr rb0, rb1;
    Tgt cur, nxt;
    lptr lds; unsigned sync_target; unsigned cnt_v, one_v;
};
template <int Q> DI void ffn_dma(const Tgt& t, int j) { dma16(t.ld[Q] + j * SLOT, t.vo, t.rs[Q], t.base[Q] + j * t.jstr); }
template <int T, int U> DI void ffn_read1(Ffn& f) {
    constexpr int j = T / 6, kk = T % 6; lptr b = (j < 3 ? f.rb0 : f.rb1) + (j % 3) * SLOT + kk * 1024 + U * 6144;
    if constexpr (U) f.au[T % 2] = trfrag(b, 256); else f.ag[T % 2] = trfrag(b, 256);
}
template <int T> DI void ffn_issue(Ffn& f) {
    constexpr int j = T / 6, kk = T % 6;
    if constexpr (kk == 4 || kk == 0 || kk == 2) {
        constexpr int q = kk == 4 ? 0 : kk == 0 ? 1 : 2; constexpr int m = (kk == 4 ? j : j - 1) + 5;
        if constexpr (m < 6) ffn_dma<q>(f.cur, m); else ffn_dma<q>(f.nxt, m - 6);
    }
}
template <int T> DI void ffn_norm(Ffn& f, f32x2 r2, f32x2 c2) {
    float lo[4], hi[4];
#pragma unroll
    for (int i = 0; i < 4; ++i) { lo[i] = blo(f.xf[T][i]); hi[i] = bhi(f.xf[T][i]); }
    SBAR();
    f32x2 t[4];
#pragma unroll
    for (int i = 0; i < 4; ++i) t[i] = __builtin_elementwise_fma((f32x2){lo[i], hi[i]}, r2, c2);
    SBAR();
#pragma unroll
    for (int i = 0; i < 4; ++i) f.xf[T][i] = cvtpk(t[i][0], t[i][1]);
    PIN(f.xf[T]);
}
template <bool FIRST, int T> DI void ffn_step(Ffn& f, f32x16& g, f32x16& u, unsigned& polled, f32x2 r2, f32x2 c2, const f32x16& pg, const f32x16& pu, u32x4& w0, u32x4& w1, lptr hp) {
    constexpr int kk = T % 6;
    if constexpr (kk == 1) {
        WAITV(8);
        lds_add1(f.cnt_v + C_RING, f.one_v);
        f.sync_target += 4;
    }
    if constexpr (kk == 3) polled = lds_ld(f.lds + L_CNT + C_RING);
    if constexpr (kk == 4) {
        if (__builtin_expect((unsigned)__builtin_amdgcn_readfirstlane(polled) < f.sync_target, 0)) {
            for (unsigned n = 0; (unsigned)__builtin_amdgcn_readfirstlane(lds_ld(f.lds + L_CNT + C_RING)) < f.sync_target && n < (1u << 22); ++n) __builtin_amdgcn_s_sleep(1); }
        FENCE();
    }
    ffn_read1<(T + 1) % 36, 0>(f);
    SBAR();
    if constexpr (T == 0) g = MFMA32(f.ag[0], f.xf[0], ZERO16); else g = MFMA32(f.ag[T % 2], f.xf[T], g);
    ffn_issue<T>(f);
    SBAR();
    ffn_read1<(T + 1) % 36, 1>(f);
    SBAR();
    if constexpr (T == 0) u = MFMA32(f.au[0], f.xf[0], ZERO16); else u = MFMA32(f.au[T % 2], f.xf[T], u);
    if constexpr (FIRST && T + 2 < 36) ffn_norm<T + 2>(f, r2, c2);
    if constexpr (!FIRST && T >= 2 && T < 10) { constexpr int i = T - 2;
        const unsigned v = cvtpk(__builtin_fmaxf(pg[2 * i], 0.f) * pu[2 * i], __builtin_fmaxf(pg[2 * i + 1], 0.f) * pu[2 * i + 1]);
        if constexpr (i < 4) { w0[i] = v; PIN(w0); } else { w1[i - 4] = v; PIN(w1); }
        if constexpr (i == 3) *(LAS u32x4*)hp = w0;
        if constexpr (i == 7) *(LAS u32x4*)(hp + 1024) = w1; }
    SBAR();
}
template <bool FIRST, int... Ts> DI void ffn_chunk_(Ffn& f, f32x16& g, f32x16& u, f32x2 r2, f32x2 c2, const f32x16& pg, const f32x16& pu, lptr hp, std::integer_sequence<int, Ts...>) { unsigned polled = 0; u32x4 w0, w1; (ffn_step<FIRST, Ts>(f, g, u, polled, r2, c2, pg, pu, w0, w1, hp), ...); }

DI void ffn_path(lptr lds, const Params& P, int mt, int s, int w, int lane) {
    Ffn f; f.lds = lds;
    const int h = lane >> 5, g4 = lane >> 4, q = (lane & 15) >> 2, p = lane & 3;
    const unsigned ring = (unsigned)(uintptr_t)(lds + L_RING);
    const __amdgpu_buffer_rsrc_t rsG = mkrsrc(P.wg, DM * FF * 2), rsU = mkrsrc(P.wu, DM * FF * 2), rs1 = mkrsrc(w < 2 ? P.wg : P.wu, DM * FF * 2);
    { const __amdgpu_buffer_rsrc_t rsQ = mkrsrc(P.wq, DM * DM * 2);
#pragma unroll
      for (int i = 0; i < 3; ++i) { const int pi = w + 4 * i; const bool up = pi >= 6; const int pr = up ? pi - 6 : pi; const unsigned row = 16u * pr;
          f.cur.ld[i] = ring + (up ? 6144u : 0u) + pr * 1024u; f.cur.rs[i] = rsQ; f.cur.base[i] = row * 1152u + (unsigned)s * 72u + (up ? 36u : 0u);
          f.nxt.ld[i] = f.cur.ld[i]; f.nxt.base[i] = row * 9216u + (unsigned)s * 576u; }
      f.cur.jstr = 96u * 1152u; f.cur.vo = (lane >> 2) * 1152 + (lane & 3) * 16;
      f.nxt.rs[0] = rsG; f.nxt.rs[1] = rs1; f.nxt.rs[2] = rsU; f.nxt.jstr = 96u * 9216u; f.nxt.vo = (lane >> 2) * 9216 + (lane & 3) * 16; }
    f.rb0 = lds + L_RING + (8 * h + q) * 64 + (g4 & 1) * 32 + 8 * p; { lptr t1 = f.rb0 + 3 * SLOT; PIN(t1); f.rb1 = t1; }
    { unsigned cv = (unsigned)(uintptr_t)(lds + L_CNT), ov = 1u; PIN(cv); PIN(ov); f.cnt_v = cv; f.one_v = ov; }
    f32x16 sA = {}, sB = {}; f32x2 r2, c2;
    {
        const int r = lane & 31; const unsigned gx = (r >> 1) & 7; lptr xb = lds + L_X + (32 * w + r) * 1152;
        lptr xb4[4];
#pragma unroll
        for (int i = 0; i < 4; ++i) xb4[i] = xb + 16 * ((2 * i + h) ^ gx);
#pragma unroll
        for (int t = 0; t < 36; ++t) f.xf[t] = *(LAS u32x4*)(xb4[t & 3] + 128 * (t >> 2));
    }
    const u32x4 ones = {0x3F803F80u, 0x3F803F80u, 0x3F803F80u, 0x3F803F80u};
#pragma unroll
    for (int t = 0; t < 16; ++t) { sA = MFMA32(ones, f.xf[t], sA); sB = MFMA32(f.xf[t], f.xf[t], sB); PIN(sA); PIN(sB); SBAR(); }
    BARRIER();
#define STAT(t) do { sA = MFMA32(ones, f.xf[t], sA); sB = MFMA32(f.xf[t], f.xf[t], sB); PIN(sA); PIN(sB); SBAR(); } while (0)
    STAT(16); STAT(17); STAT(18);
    ffn_dma<0>(f.cur, 1); STAT(19); ffn_dma<1>(f.cur, 1); STAT(20); ffn_dma<2>(f.cur, 1); STAT(21);
    ffn_dma<0>(f.cur, 2); STAT(22); ffn_dma<1>(f.cur, 2); STAT(23); ffn_dma<2>(f.cur, 2); STAT(24);
    ffn_dma<0>(f.cur, 3); STAT(25); ffn_dma<1>(f.cur, 3); STAT(26); ffn_dma<2>(f.cur, 3); STAT(27);
    ffn_dma<0>(f.cur, 4); STAT(28); STAT(29); STAT(30); STAT(31); STAT(32); STAT(33); STAT(34); STAT(35);
#undef STAT
    {
        const float rowsum = sA[0];
        const int r = lane & 31, reg = (r & 3) + 4 * (r >> 3);
        float dg = sB[0];
#pragma unroll
        for (int i = 1; i < 16; ++i) dg = reg == i ? sB[i] : dg;
        dg = ((r >> 2) & 1) == h ? dg : 0.f;
        auto sw = __builtin_amdgcn_permlane32_swap(__float_as_uint(dg), __float_as_uint(dg), false, false);
        const float sq = __uint_as_float(sw[0]) + __uint_as_float(sw[1]);
        const float mean = rowsum * (1.f / DM); float var = fmaxf(sq * (1.f / DM) - mean * mean, 0.f);
        if (__builtin_expect(__any(mean * mean > 256.f * var), 0)) {
            float v2 = 0.f; const u32x4* xr = (const u32x4*)(P.x + (size_t)(128 * mt + 32 * w + r) * DM) + h;
#pragma unroll 1
            for (int t = 0; t < 36; ++t) { const u32x4 xv = xr[2 * t];
#pragma unroll
                for (int i = 0; i < 4; ++i) { const float d0 = blo(xv[i]) - mean, d1 = bhi(xv[i]) - mean; v2 = __builtin_fmaf(d0, d0, v2); v2 = __builtin_fmaf(d1, d1, v2); } }
            auto s2 = __builtin_amdgcn_permlane32_swap(__float_as_uint(v2), __float_as_uint(v2), false, false);
            var = (__uint_as_float(s2[0]) + __uint_as_float(s2[1])) * (1.f / DM);
        }
        const float mb = bfr(mean), sd = bfr(__builtin_amdgcn_sqrtf(bfr(bfr(var) + 1e-5f)));
        const float rinv = __builtin_amdgcn_rcpf(sd), cc = -mb * rinv; r2 = (f32x2){rinv, rinv}; c2 = (f32x2){cc, cc};
    }
    PIN(r2); PIN(c2);
#pragma unroll
    for (int t = 0; t < 36; ++t) PIN(f.xf[t]);
    ffn_norm<0>(f, r2, c2); ffn_norm<1>(f, r2, c2);
    f.sync_target = 0;
    ffn_read1<0, 0>(f); ffn_read1<0, 1>(f);
    __builtin_amdgcn_s_setprio(FFN_PRIO);
    auto next_params = [&](int c) {
        f.cur = f.nxt;
        if (c < 8) {
#pragma unroll
            for (int i = 0; i < 3; ++i) f.nxt.base[i] += 64u;
        } else if (c == 8) {
            const __amdgpu_buffer_rsrc_t rsD = mkrsrc(P.wd, FF * DM * 2);
#pragma unroll
            for (int i = 0; i < 3; ++i) { f.nxt.rs[i] = rsD; f.nxt.ld[i] = ring + 1024u * (w + 4 * i); f.nxt.base[i] = (288u * s + 8u * (w + 4 * i)) * 1152u; }
            f.nxt.jstr = 96u * 1152u; f.nxt.vo = (lane >> 3) * 1152 + (((lane & 7) ^ (((lane >> 4) & 1) << 2)) * 16);
        }
    };
    {
        f32x16 g, u;
        ffn_chunk_<true>(f, g, u, r2, c2, g, u, lds, std::make_integer_sequence<int, 36>{});
        const bool lo = lane < 32;
#pragma unroll
        for (int a = 0; a < 2; ++a) { const f32x16& qq = a ? u : g; u32x4 f0, f1;
#pragma unroll
            for (int i = 0; i < 4; ++i) f0[i] = cvtpk(qq[2 * i] * QSCALE, qq[2 * i + 1] * QSCALE);
            f1 = (u32x4){lo ? cvtpk(qq[8] * QSCALE, qq[9] * QSCALE) : 0u, 0u, 0u, 0u};
            *(LAS u32x4*)(lds + L_Q + (w * 2 + a) * 2048 + lane * 16) = f0;
            *(LAS u32x4*)(lds + L_Q + (w * 2 + a) * 2048 + 1024 + lane * 16) = f1; }
        WAITL0();
        lds_add1(f.cnt_v + C_Q, f.one_v);
        next_params(0);
    }
    lptr hw = lds + L_H + w * 2048 + lane * 16;
    f32x16 gA, uA, gB = {}, uB = {};
    for (int c = 1; c < 10; c += 2) {
        if (c == 9) lds_wait_ge(lds + L_CNT + C_QTAKEN, 4);
        ffn_chunk_<false>(f, gA, uA, r2, c2, gB, uB, c == 1 ? lds + L_O + w * 2048 + lane * 16 : hw + (c - 2) * 8192, std::make_integer_sequence<int, 36>{});
        next_params(c);
        if (c + 1 < 10) {
            if (c == 1) lds_wait_ge(lds + L_CNT + C_KV2, 4);
            ffn_chunk_<false>(f, gB, uB, r2, c2, gA, uA, hw + (c - 1) * 8192, std::make_integer_sequence<int, 36>{});
            next_params(c + 1);
        }
    }
    {
        u32x4 w0, w1;
#pragma unroll
        for (int i = 0; i < 4; ++i) { w0[i] = cvtpk(__builtin_fmaxf(gA[2 * i], 0.f) * uA[2 * i], __builtin_fmaxf(gA[2 * i + 1], 0.f) * uA[2 * i + 1]);
                                      w1[i] = cvtpk(__builtin_fmaxf(gA[8 + 2 * i], 0.f) * uA[8 + 2 * i], __builtin_fmaxf(gA[9 + 2 * i], 0.f) * uA[9 + 2 * i]); }
        *(LAS u32x4*)(hw + 8 * 8192) = w0; *(LAS u32x4*)(hw + 8 * 8192 + 1024) = w1;
    }
    __builtin_amdgcn_s_setprio(0);
    WAITV(0);
}

struct KVf { u32x4 k1, k2, v0, v1; };
#define EX(x) __builtin_amdgcn_exp2f(x)
#define APRIO_HI() __builtin_amdgcn_s_setprio(ATT_TOGGLE)
#define APRIO_LO() __builtin_amdgcn_s_setprio(0)
DI void att_slot(f32x16& Sc, f32x16& Sn, u32x4& pup, const u32x4& k1, const u32x4& k2, const u32x4& q1, const u32x4& q2,
                 const u32x4& v1p, f32x16& Op, const u32x4& v0c, f32x16& Oc) {
    u32x4 plo;
    pup[2] = cvtpk(Sn[12], Sn[13]); pup[3] = cvtpk(Sn[14], Sn[15]); PIN(pup); SBAR();
    APRIO_HI(); Sn = MFMA32(k1, q1, ZERO16); SBAR(); APRIO_LO();
    Sc[0] = EX(Sc[0]); Sc[1] = EX(Sc[1]); Sc[2] = EX(Sc[2]); Sc[3] = EX(Sc[3]); PIN(Sc); SBAR();
    APRIO_HI(); Op = MFMA32(v1p, pup, Op); SBAR(); APRIO_LO();
    Sc[4] = EX(Sc[4]); Sc[5] = EX(Sc[5]); Sc[6] = EX(Sc[6]); Sc[7] = EX(Sc[7]); plo[0] = cvtpk(Sc[0], Sc[1]); plo[1] = cvtpk(Sc[2], Sc[3]); PIN(Sc); PIN(plo); SBAR();
    APRIO_HI(); Sn = MFMA32(k2, q2, Sn); SBAR(); APRIO_LO();
    Sc[8] = EX(Sc[8]); Sc[9] = EX(Sc[9]); Sc[10] = EX(Sc[10]); Sc[11] = EX(Sc[11]); plo[2] = cvtpk(Sc[4], Sc[5]); plo[3] = cvtpk(Sc[6], Sc[7]); PIN(Sc); PIN(plo); SBAR();
    Sc[12] = EX(Sc[12]); Sc[13] = EX(Sc[13]); SBAR();
    APRIO_HI(); Oc = MFMA32(v0c, plo, Oc); SBAR(); APRIO_LO();
    Sc[14] = EX(Sc[14]); Sc[15] = EX(Sc[15]); pup[0] = cvtpk(Sc[8], Sc[9]); pup[1] = cvtpk(Sc[10], Sc[11]); PIN(Sc); PIN(pup); SBAR();
}
DI void att_path(lptr lds, const Params& P, const KvPre& kpre, int mt, int s, int a4, int lane, u64 tag, int bid) {
    unsigned cnt_v = (unsigned)(uintptr_t)(lds + L_CNT), one_v = 1u; PIN(cnt_v); PIN(one_v);
    kv_job(lds, kpre, a4, lane, mt, s, P, tag, cnt_v, one_v, bid);
    {
        const int s0 = s & ~(XMAP - 1); constexpr int nl = XMAP * 324 * 9, per = (nl + 8191) / 8192; unsigned sink = 0u;
#pragma unroll
        for (int i = 0; i < per; ++i) { const int id = ((bid >> 3) * per + i) * 256 + a4 * 64 + lane;
            if (id < nl) { const int sl = id / 2916, rem = id - 2916 * sl;
                const char* ptr = rem < 2592 ? (const char*)P.wd + (size_t)(288 * (s0 + sl)) * 1152 + rem * 128 : (const char*)P.wo + (size_t)(36 * (s0 + sl)) * 1152 + (rem - 2592) * 128;
                sink ^= *(const unsigned*)ptr; } }
        asm volatile("" :: "v"(sink));
    }
    if (a4 == 0) { wait_flags<4, KV_SLEEP>(P.ws + WS_FB + (bid & 7) * 2048, 64, tag, lane);
        lds_add1(cnt_v + C_KVOK, one_v);
        if (bid == 0 && lane == 0) gst64(P.ws + WS_G, tag); }
    else lds_wait_ge(lds + L_CNT + C_KVOK, 1);
    const int rh = a4 & 1, kh = a4 >> 1, r = lane & 31, h = lane >> 5;
    const __amdgpu_buffer_rsrc_t rs = mkrsrc(P.ws, WS_KV_END);
    const unsigned voK1 = WS_K1 + lane * 16, voK2 = h == 0 ? WS_K2 + r * 16 : OOB, voV = r < 19 ? WS_V + h * 304 + r * 16 : OOB;
#define LDK(B, kb) do { B.k1 = __builtin_amdgcn_raw_buffer_load_b128(rs, voK1, (kb) * 1024, LDAUX); B.k2 = __builtin_amdgcn_raw_buffer_load_b128(rs, voK2, (kb) * 512, LDAUX); } while (0)
#define LDV(B, kb) do { B.v0 = __builtin_amdgcn_raw_buffer_load_b128(rs, voV, (kb) * 1216, LDAUX); B.v1 = __builtin_amdgcn_raw_buffer_load_b128(rs, voV, (kb) * 1216 + 608, LDAUX); } while (0)
    KVf A, B;
    const int kb0 = 32 * kh;
    KVf R; LDK(R, 0);
    LDK(A, kb0); LDV(A, kb0); LDK(B, kb0 + 1);
    lds_wait_ge(lds + L_CNT + C_Q, 4);
    u32x4 q1[4], q2[4];
#pragma unroll
    for (int T = 0; T < 4; ++T) { const int rt = 2 * rh + (T >> 1), a = T & 1;
        q1[T] = *(LAS u32x4*)(lds + L_Q + (rt * 2 + a) * 2048 + lane * 16); q2[T] = *(LAS u32x4*)(lds + L_Q + (rt * 2 + a) * 2048 + 1024 + lane * 16); }
    WAITL0();
#pragma unroll
    for (int T = 0; T < 4; ++T) { PIN(q1[T]); PIN(q2[T]); }
    lds_add1(cnt_v + C_QTAKEN, one_v);
    float mref[4];
#pragma unroll
    for (int T = 0; T < 4; ++T) {
        f32x16 s0 = MFMA32(R.k1, q1[T], ZERO16); s0 = MFMA32(R.k2, q2[T], s0);
        float m = s0[0];
#pragma unroll
        for (int i = 1; i < 16; ++i) m = fmaxf(m, s0[i]);
        m = swap32_max(m); const unsigned mb = cvtpk(-m, 0.f) & 0xffffu; mref[T] = -__uint_as_float(mb << 16);
        if (h == 0) q2[T][1] = mb;
    }
    f32x16 O[4] = {}; f32x16 S0, S1 = {}; u32x4 pup = {0u, 0u, 0u, 0u};
    B.v1 = A.v1;
    S0 = MFMA32(A.k1, q1[0], ZERO16); S0 = MFMA32(A.k2, q2[0], S0);
    for (int it = 0; it < 16; ++it) {
        const int kb = kb0 + 2 * it;
        att_slot(S0, S1, pup, A.k1, A.k2, q1[1], q2[1], B.v1, O[3], A.v0, O[0]);
        LDV(B, kb + 1);
        att_slot(S1, S0, pup, A.k1, A.k2, q1[2], q2[2], A.v1, O[0], A.v0, O[1]);
        att_slot(S0, S1, pup, A.k1, A.k2, q1[3], q2[3], A.v1, O[1], A.v0, O[2]);
        { const int kn = (kb + 2 < 64) ? kb + 2 : 63; LDK(A, kn); }
        att_slot(S1, S0, pup, B.k1, B.k2, q1[0], q2[0], A.v1, O[2], A.v0, O[3]);
        att_slot(S0, S1, pup, B.k1, B.k2, q1[1], q2[1], A.v1, O[3], B.v0, O[0]);
        { const int kn = (kb + 2 < 64) ? kb + 2 : 63; LDV(A, kn); }
        att_slot(S1, S0, pup, B.k1, B.k2, q1[2], q2[2], B.v1, O[0], B.v0, O[1]);
        att_slot(S0, S1, pup, B.k1, B.k2, q1[3], q2[3], B.v1, O[1], B.v0, O[2]);
        { const int kn = (kb + 3 < 64) ? kb + 3 : 63; LDK(B, kn); }
        att_slot(S1, S0, pup, A.k1, A.k2, q1[0], q2[0], B.v1, O[2], B.v0, O[3]);
    }
    pup[2] = cvtpk(S1[12], S1[13]); pup[3] = cvtpk(S1[14], S1[15]);
    O[3] = MFMA32(B.v1, pup, O[3]);
    {
        bool bad = false;
#pragma unroll
        for (int T = 0; T < 4; ++T) { float mx = 0.f;
#pragma unroll
            for (int i = 0; i < 10; ++i) mx = fmaxf(mx, __builtin_fabsf(O[T][i]));
            bad |= !(O[T][10] < 0x1p60f) || !(mx < 0x1p120f) ;
#pragma unroll
            for (int i = 0; i < 10; ++i) bad |= O[T][i] != O[T][i]; }
        if (__builtin_expect(__any(bad), 0)) {
#pragma unroll
            for (int T = 0; T < 4; ++T) { O[T] = ZERO16; mref[T] = -__builtin_inff(); if (h == 0) q2[T][1] = 0u; }
            for (int kb = kb0; kb < kb0 + 32; ++kb) {
                KVf C; LDK(C, kb); LDV(C, kb);
#pragma unroll
                for (int T = 0; T < 4; ++T) {
                    f32x16 sc = MFMA32(C.k1, q1[T], ZERO16); sc = MFMA32(C.k2, q2[T], sc);
                    float m = sc[0];
#pragma unroll
                    for (int i = 1; i < 16; ++i) m = fmaxf(m, sc[i]);
                    m = fmaxf(swap32_max(m), mref[T]);
                    const float al = EX(mref[T] - m); mref[T] = m;
#pragma unroll
                    for (int i = 0; i < 16; ++i) { O[T][i] *= al; sc[i] = EX(sc[i] - m); }
                    const u32x4 p0 = {cvtpk(sc[0], sc[1]), cvtpk(sc[2], sc[3]), cvtpk(sc[4], sc[5]), cvtpk(sc[6], sc[7])}, p1 = {cvtpk(sc[8], sc[9]), cvtpk(sc[10], sc[11]), cvtpk(sc[12], sc[13]), cvtpk(sc[14], sc[15])};
                    O[T] = MFMA32(C.v0, p0, O[T]); O[T] = MFMA32(C.v1, p1, O[T]);
                }
            }
        }
    }
    BARRIER();
    {
        const __amdgpu_buffer_rsrc_t rsO = mkrsrc(P.wo, DM * DM * 2); const unsigned voD = (lane >> 3) * 1152 + (((lane & 7) ^ (((lane >> 4) & 1) << 2)) * 16), ring = (unsigned)(uintptr_t)(lds + L_RING);
        dma16(ring + (36 + a4) * 1024u, voD, rsO, (unsigned)(36 * s + 8 * a4) * 1152u);
        if (a4 < 2) dma16(ring + (40 + a4) * 1024u, voD, rsO, (unsigned)(36 * s + 8 * (a4 + 4)) * 1152u); }
    auto send = [&](const f32x16& o, float m, int rr) {
        lptr d = lds + L_XCH + (a4 * 2 + rr) * 3072 + lane * 16;
        *(LAS f32x4*)(d) = (f32x4){o[0], o[1], o[2], o[3]}; *(LAS f32x4*)(d + 1024) = (f32x4){o[4], o[5], o[6], o[7]}; *(LAS f32x4*)(d + 2048) = (f32x4){o[8], o[9], o[10], m}; };
    if (kh) { send(O[0], mref[0], 0); send(O[2], mref[2], 1); } else { send(O[1], mref[1], 0); send(O[3], mref[3], 1); }
    BARRIER();
    auto comb = [&](const f32x16& o, float m, int rr) {
        lptr d = lds + L_XCH + ((a4 ^ 2) * 2 + rr) * 3072 + lane * 16;
        const f32x4 a0 = *(LAS f32x4*)(d), a1 = *(LAS f32x4*)(d + 1024), a2 = *(LAS f32x4*)(d + 2048);
        const float M = fmaxf(m, a2[3]), fa = EX(m - M), fb = EX(a2[3] - M);
        float v[10];
#pragma unroll
        for (int i = 0; i < 4; ++i) { v[i] = o[i] * fa + a0[i] * fb; v[4 + i] = o[4 + i] * fa + a1[i] * fb; }
        v[8] = o[8] * fa + a2[0] * fb; v[9] = o[9] * fa + a2[1] * fb;
        float l = o[10] * fa + a2[2] * fb;
        l = __shfl(l, r, 64);
        const float il = __builtin_amdgcn_rcpf(l);
        lptr od = lds + L_O + (64 * rh + 32 * rr + r) * O_STRIDE + 36 * kh;
        *(LAS unsigned*)(od + 8 * h) = cvtpk(v[0] * il, v[1] * il); *(LAS unsigned*)(od + 8 * h + 4) = cvtpk(v[2] * il, v[3] * il);
        *(LAS unsigned*)(od + 16 + 8 * h) = cvtpk(v[4] * il, v[5] * il); *(LAS unsigned*)(od + 16 + 8 * h + 4) = cvtpk(v[6] * il, v[7] * il);
        if (h == 0) *(LAS unsigned*)(od + 32) = cvtpk(v[8] * il, v[9] * il);
        else if (kh == 1) { *(LAS unsigned*)(od + 36) = 0u; *(LAS unsigned*)(od + 40) = 0u; *(LAS unsigned*)(od + 44) = 0u; *(LAS unsigned*)(od + 48) = 0u; *(LAS unsigned*)(od + 52) = 0u; *(LAS unsigned*)(od + 56) = 0u; }
    };
    if (kh) { comb(O[1], mref[1], 0); comb(O[3], mref[3], 1); } else { comb(O[0], mref[0], 0); comb(O[2], mref[2], 1); }
}

struct PhB { __amdgpu_buffer_rsrc_t rsD, rsO, rsP; unsigned voD, vo5, ldsw, so0, so4, so5, po, d5, d5str; lptr tb[3]; u32x4 af[7]; };
template <int NH, int NP, int I> DI void phb_piece(PhB& b) {
    constexpr unsigned col = NP * 128u; const unsigned dst = b.ldsw + (NP % 3) * BBUF;
    if constexpr (I < 4) dma16(dst + I * 8192, b.voD, b.rsD, b.so0 + I * (64u * 1152u) + col);
    else if constexpr (I == 4) { if constexpr (NH == 0) dma16(dst + I * 8192, b.voD, b.rsD, b.so4 + col); else dma16(dst + I * 8192, b.voD, b.rsO, b.so4 + col); }
    else if constexpr (NH == 0) dma16(b.d5 + (NP % 3) * b.d5str, b.vo5, b.rsO, b.so5 + col);
}
template <int NH, int NP, int KT> DI void phb_issue(PhB& b) {
    constexpr int o = KT >= 17 ? KT - 17 : KT + 4, m = KT >= 17 ? NP + 3 : NP + 2;
    if constexpr (m < 9 && o < 18 && (o % 3) == (NH ? 2 : 0)) phb_piece<NH, m, o / 3>(b);
}
template <int NP> DI void phb_store(PhB& b, const u32x4& w, int half) { __builtin_amdgcn_raw_buffer_store_b128(w, b.rsP, b.po + half * 1024, NP * 4096, SC1); }
template <int NH, int NP, int KT> DI void phb_step(PhB& b, const u32x4 (&hf)[21], f32x16& acc, f32x16& prev, u32x4& w0, u32x4& w1) {
    constexpr int P = NH ? 5 : 6;
    if constexpr (KT == 17 && NP < 8) asm volatile("s_waitcnt vmcnt(%0) lgkmcnt(0)\n\ts_barrier" :: "i"((NP + 2 < 9 ? P : 0) + (NP > 0 ? 2 : 0)) : "memory");
    if constexpr (KT + 4 < 21) b.af[(KT + 4) % 7] = trfrag(b.tb[NP % 3] + (KT + 4) * 2048, 1024); else if constexpr (NP < 8) b.af[(KT + 4) % 7] = trfrag(b.tb[(NP + 1) % 3] + (KT + 4 - 21) * 2048, 1024);
    SBAR();
    if constexpr (KT == 0) acc = MFMA32(b.af[0], hf[0], ZERO16); else acc = MFMA32(b.af[KT % 7], hf[KT], acc);
    phb_issue<NH, NP, KT>(b);
    if constexpr (NP > 0 && KT >= 2 && KT < 6) { constexpr int i = KT - 2;
        if constexpr (i < 2) { w0[2 * i] = cvtpk(prev[4 * i], prev[4 * i + 1]); w0[2 * i + 1] = cvtpk(prev[4 * i + 2], prev[4 * i + 3]); PIN(w0); }
        else { w1[2 * i - 4] = cvtpk(prev[4 * i], prev[4 * i + 1]); w1[2 * i - 3] = cvtpk(prev[4 * i + 2], prev[4 * i + 3]); PIN(w1); }
        if constexpr (i == 1) phb_store<NP - 1>(b, w0, 0);
        if constexpr (i == 3) phb_store<NP - 1>(b, w1, 1);
    }
    SBAR();
}
template <int NH, int NP, int... Ks> DI void phb_stage_(PhB& b, const u32x4 (&hf)[21], f32x16& acc, f32x16& prev, std::integer_sequence<int, Ks...>) {
    u32x4 w0, w1; (phb_step<NH, NP, Ks>(b, hf, acc, prev, w0, w1), ...);
}
template <int NH, int NP> DI void phb_stage(PhB& b, const u32x4 (&hf)[21], f32x16& acc, f32x16& prev) { phb_stage_<NH, NP>(b, hf, acc, prev, std::make_integer_sequence<int, 21>{}); }
template <int NH> DI void phb_run(PhB& b, const u32x4 (&hf)[21], bool early, char* eflag, u64 tag) {
    b.af[0] = trfrag(b.tb[0], 1024); b.af[1] = trfrag(b.tb[0] + 2048, 1024); b.af[2] = trfrag(b.tb[0] + 4096, 1024); b.af[3] = trfrag(b.tb[0] + 6144, 1024);
    phb_piece<NH, 1, 0>(b); phb_piece<NH, 1, 1>(b); phb_piece<NH, 1, 2>(b); phb_piece<NH, 1, 3>(b); phb_piece<NH, 1, 4>(b); phb_piece<NH, 1, 5>(b);
    phb_piece<NH, 2, 0>(b);
    if constexpr (NH == 0) phb_piece<NH, 2, 1>(b);
    f32x16 a0, a1;
    phb_stage<NH, 0>(b, hf, a0, a1); phb_stage<NH, 1>(b, hf, a1, a0); phb_stage<NH, 2>(b, hf, a0, a1); phb_stage<NH, 3>(b, hf, a1, a0);
    phb_stage<NH, 4>(b, hf, a0, a1); phb_stage<NH, 5>(b, hf, a1, a0); phb_stage<NH, 6>(b, hf, a0, a1); phb_stage<NH, 7>(b, hf, a1, a0);
    if (early) gst64(eflag, tag);
    phb_stage<NH, 8>(b, hf, a0, a1);
    u32x4 w0, w1;
#pragma unroll
    for (int i = 0; i < 4; ++i) { w0[i] = cvtpk(a0[2 * i], a0[2 * i + 1]); w1[i] = cvtpk(a0[8 + 2 * i], a0[9 + 2 * i]); }
    phb_store<8>(b, w0, 0); phb_store<8>(b, w1, 1);
}

__global__ __launch_bounds__(512, 2) void fused_kernel(Params P) {
    extern __shared__ __attribute__((aligned(16))) char smem[];
    lptr lds = (lptr)smem;
    const int tid = threadIdx.x, lane0 = tid & 63, wid = __builtin_amdgcn_readfirstlane(tid >> 6);
    const int bid = blockIdx.x;
    const int xg = bid & 7, jj = bid >> 3, s = 4 * (xg & 3) + (jj & 3), mt = 8 * (xg >> 2) + (jj >> 2);
    const u64 g0 = gld64(P.ws + WS_G);
    if (tid < 16) *(LAS unsigned*)(lds + L_CNT + 4 * tid) = 0u;
    unsigned codepf = 0u;
    {
        const char* pc = (const char*)__builtin_amdgcn_s_getpc();
        if (tid < CODEPF) codepf = *(const GAS unsigned*)(pc + tid * 128); }
    {
        const __amdgpu_buffer_rsrc_t rsX = mkrsrc(P.x, SEQ * DM * 2);
        const unsigned ldsu0 = (unsigned)(uintptr_t)(lds + L_X) + wid * 18432u, so = (unsigned)(128 * mt + 16 * wid) * 1152u;
        unsigned row = 0u, cp = lane0;
#pragma unroll 2
        for (int i = 0; i < 18; ++i) {
            dma16(ldsu0 + i * 1024, row * 1152u + 16u * ((cp & ~7u) | ((cp & 7u) ^ ((row >> 1) & 7u))), rsX, so);
            cp += 64u; if (cp >= 72u) { cp -= 72u; row += 1u; } }
    }
    if (wid >= 4) {
        const int w = wid & 3; const __amdgpu_buffer_rsrc_t rsQ = mkrsrc(P.wq, DM * DM * 2);
        const unsigned vq = (lane0 >> 2) * 1152 + (lane0 & 3) * 16, ring = (unsigned)(uintptr_t)(lds + L_RING);
#pragma unroll
        for (int i = 0; i < 3; ++i) { const int pi = w + 4 * i; const bool up = pi >= 6; const int pr = up ? pi - 6 : pi;
            dma16(ring + (up ? 6144u : 0u) + pr * 1024u, vq, rsQ, 16u * pr * 1152u + (unsigned)s * 72u + (up ? 36u : 0u)); }
    }
    KvPre kpre;
    if (wid < 4) kv_preload(kpre, tid, P);
    const u64 tag = g0 + 1ull;
    if (wid < 4) WAITV(11); else WAITV(0);
    asm volatile("" :: "v"(codepf));
    BARRIER();
    u32x4 hf[21];
    if (wid >= 4) { ffn_path(lds, P, mt, s, wid & 3, lane0); BARRIER();
#pragma unroll
        for (int t = 0; t < 18; ++t) hf[t] = *(LAS u32x4*)(lds + L_H + ((t >> 1) * 4 + (wid & 3)) * 2048 + (t & 1) * 1024 + lane0 * 16);
        BARRIER(); }
    else att_path(lds, P, kpre, mt, s, wid & 3, lane0, tag, bid);
    int tz = threadIdx.x; asm volatile("" : "+v"(tz));
    const int lane = tz & 63;
    const int rt = wid & 3, nh = wid >> 2, r = lane & 31, h = lane >> 5, g4 = lane >> 4, q = (lane & 15) >> 2, p = lane & 3;
    PhB b;
    b.rsD = mkrsrc(P.wd, FF * DM * 2); b.rsO = mkrsrc(P.wo, DM * DM * 2);
    b.voD = (lane >> 3) * 1152 + (((lane & 7) ^ (((lane >> 4) & 1) << 2)) * 16); b.vo5 = wid < 2 ? b.voD : OOB;
    b.ldsw = (unsigned)(uintptr_t)(lds + L_RING) + wid * 1024;
    b.d5 = wid < 2 ? b.ldsw + 5 * 8192 : (unsigned)(uintptr_t)(lds + L_RING) + 3 * BBUF; b.d5str = wid < 2 ? BBUF : 0u;
    b.so0 = (unsigned)(288 * s + 8 * wid) * 1152u;
    b.so4 = wid < 4 ? (unsigned)(288 * s + 8 * (wid + 32)) * 1152u : (unsigned)(36 * s + 8 * (wid - 4)) * 1152u;
    b.so5 = (unsigned)(36 * s + 8 * (wid + 4)) * 1152u;
    const unsigned rowl = 4 * h + q; const unsigned ch = (4 * nh + 2 * (g4 & 1) + (p >> 1)) ^ (((rowl >> 1) & 1) << 2);
    b.tb[0] = lds + L_RING + rowl * 128 + ch * 16 + 8 * (p & 1);
    { lptr t1 = b.tb[0] + BBUF, t2 = b.tb[0] + 2 * BBUF; PIN(t1); PIN(t2); b.tb[1] = t1; b.tb[2] = t2; }
    b.rsP = mkrsrc(P.ws + WS_PART + (size_t)(mt * 16 + s) * SLAB, SLAB);
    b.po = (unsigned)(rt * 18 + nh) * 2048u + lane * 16;
    BARRIER();
    if (wid < 4) {
#pragma unroll
        for (int t = 0; t < 18; ++t) hf[t] = *(LAS u32x4*)(lds + L_H + ((t >> 1) * 4 + rt) * 2048 + (t & 1) * 1024 + lane * 16); }
#pragma unroll
    for (int t = 0; t < 3; ++t) { lptr o = lds + L_O + (32 * rt + r) * O_STRIDE + 32 * t + 8 * h; const u32x2 a = *(LAS u32x2*)o, bb = *(LAS u32x2*)(o + 16); hf[18 + t] = (u32x4){a[0], a[1], bb[0], bb[1]}; }
    WAITL0();
#pragma unroll
    for (int t = 0; t < 21; ++t) PIN(hf[t]);
    WAITV(0);
    BARRIER();
    { const bool early = wid == 0 && lane == 0; char* ef = P.ws + WS_FE + mt * 128 + s * 8;
      if (nh) phb_run<1>(b, hf, early, ef, tag); else phb_run<0>(b, hf, early, ef, tag); }
    const int urt = s >> 2, unh = (s >> 1) & 1, uhf = s & 1;
    const unsigned uo0 = (unsigned)(urt * 18 + 2 * wid + unh) * 2048u + uhf * 1024u, uo8 = (unsigned)(urt * 18 + 16 + unh) * 2048u + uhf * 1024u;
    const size_t ro0 = (size_t)(128 * mt + 32 * urt + r) * DM + 32 * (2 * wid + unh) + 16 * uhf + 4 * h;
    const u32x2 x0 = *(const u32x2*)(P.x + ro0), x1 = *(const u32x2*)(P.x + ro0 + 8);
    const int l8 = 8 * wid + (lane & 7);
    const size_t ro8 = (size_t)(128 * mt + 32 * urt + (l8 & 31)) * DM + 32 * (16 + unh) + 16 * uhf + 4 * (l8 >> 5);
    u32x2 y0 = {0u, 0u}, y1 = {0u, 0u};
    if (lane < 8) { y0 = *(const u32x2*)(P.x + ro8); y1 = *(const u32x2*)(P.x + ro8 + 8); }
    WAITV(0);
    BARRIER();
    if (wid == 0 && lane == 0) gst64(P.ws + WS_FC + mt * 128 + s * 8, tag);
    {   const __amdgpu_buffer_rsrc_t rp = mkrsrc(P.ws + WS_PART + (size_t)mt * 16 * SLAB, 16 * SLAB);
        u32x4 v[16], v8[16]; const unsigned vo8 = lane < 8 ? l8 * 16u : OOB;
        if (wid == 0) wait_flags<1, 2>(P.ws + WS_FE + mt * 128, 16, tag, lane);
        BARRIER();
        if (wid < 6) {
#pragma unroll
            for (int k = 0; k < 16; ++k) v[k] = __builtin_amdgcn_raw_buffer_load_b128(rp, lane * 16, k * SLAB + uo0, SC1); }
        if (wid == 7) wait_flags<1, 2>(P.ws + WS_FC + mt * 128, 16, tag, lane);
        BARRIER();
        if (wid >= 6) {
#pragma unroll
            for (int k = 0; k < 16; ++k) v[k] = __builtin_amdgcn_raw_buffer_load_b128(rp, lane * 16, k * SLAB + uo0, SC1); }
#pragma unroll
        for (int k = 0; k < 16; ++k) v8[k] = __builtin_amdgcn_raw_buffer_load_b128(rp, vo8, k * SLAB + uo8, SC1);
        auto fin = [&](const u32x4 (&vv)[16], const u32x2& xa, const u32x2& xb, size_t ro, bool on) {
            float acc[8] = {blo(xa[0]), bhi(xa[0]), blo(xa[1]), bhi(xa[1]), blo(xb[0]), bhi(xb[0]), blo(xb[1]), bhi(xb[1])};
#pragma unroll
            for (int k = 0; k < 16; ++k)
#pragma unroll
                for (int i = 0; i < 4; ++i) { acc[2 * i] += blo(vv[k][i]); acc[2 * i + 1] += bhi(vv[k][i]); }
            if (on) { *(u32x2*)(P.out + ro) = (u32x2){cvtpk(acc[0], acc[1]), cvtpk(acc[2], acc[3])};
                      *(u32x2*)(P.out + ro + 8) = (u32x2){cvtpk(acc[4], acc[5]), cvtpk(acc[6], acc[7])}; } };
        fin(v, x0, x1, ro0, true);
        fin(v8, y0, y1, ro8, lane < 8);
    }
}

extern "C" void kernel_launch(void* const* d_in, const int* in_sizes, int n_in, void* d_out, int out_size, void* d_ws, size_t ws_size, hipStream_t stream) {
    Params P; P.x = (const u16*)d_in[0]; P.wq = (const u16*)d_in[1]; P.wk = (const u16*)d_in[2]; P.wv = (const u16*)d_in[3]; P.wo = (const u16*)d_in[4];
    P.wg = (const u16*)d_in[6]; P.wu = (const u16*)d_in[7]; P.wd = (const u16*)d_in[8]; P.out = (u16*)d_out; P.ws = (char*)d_ws;
    (void)hipFuncSetAttribute((const void*)fused_kernel, hipFuncAttributeMaxDynamicSharedMemorySize, LDS_BYTES);
    fused_kernel<<<256, 512, LDS_BYTES, stream>>>(P);
}
```
